```python
import torch

def _softmax(x, axis=-1):
    return torch.softmax(x, dim=axis)

def _gelu(x):
    return torch.nn.functional.gelu(x, approximate='tanh')

def _silu(x):
    return torch.nn.functional.silu(x)

def _sigmoid(x):
    return torch.sigmoid(x)

def _layernorm(x, axis=-1, eps=1e-05):
    m = x.mean(dim=axis, keepdim=True)
    v = x.var(dim=axis, keepdim=True, unbiased=False)
    return (x - m) / torch.sqrt(v + eps)

def _scatter_add(tgt, idx, src, axis=0):
    return tgt.clone().index_add_(axis, idx, src)

def _segment_sum(src, seg, n):
    out = torch.zeros((n,) + src.shape[1:], dtype=src.dtype)
    return out.index_add_(0, seg, src)

def _pad(x, pw):
    flat = tuple((v for pair in reversed(pw) for v in pair))
    return torch.nn.functional.pad(x, flat)
kernel_name = 'sequential_preLN__memory_aug_recurrent_mem_tr__mixer_s_mlp__prompt'

def setup_inputs():

    _w = lambda a: (lambda t: t.to(torch.bfloat16) if t.is_floating_point() else t)(torch.as_tensor(a))

    def _A_setup():
        B, S, Dm = (1, 2048, 512)
        H, Dh = (32, 128)
        M, seg = (8, 32)
        seg = min(seg, S)
        S = S // seg * seg
        return {'x': _w((lambda *s: torch.randn(*s, dtype=torch.bfloat16) * 0.05)(B, S, Dm)), 'mem0': _w((lambda *s: torch.randn(*s, dtype=torch.bfloat16) * 0.2)(B, M, Dm)), 'Wq': _w((lambda *s: torch.randn(*s, dtype=torch.bfloat16) * 1.6)(Dm, H, Dh) * Dm ** (-0.5)), 'Wk': _w((lambda *s: torch.randn(*s, dtype=torch.bfloat16) * 1.6)(Dm, H, Dh) * Dm ** (-0.5)), 'Wv': _w((lambda *s: torch.randn(*s, dtype=torch.bfloat16) * 0.2)(Dm, H, Dh) * Dm ** (-0.5)), 'Wo': _w((lambda *s: torch.randn(*s, dtype=torch.bfloat16) * 0.2)(H, Dh, Dm) * (H * Dh) ** (-0.5)), 'seg_len': seg}
    _A_in = _A_setup()

    def _B_setup():
        B, S, D, F = (1, 2048, 512, 1792)
        return {'x': _w((lambda *s: torch.randn(*s, dtype=torch.bfloat16) * 0.05)(B, S, D)), 'w1': _w((lambda *s: torch.randn(*s, dtype=torch.bfloat16) * 0.8)(D, D) * D ** (-0.5)), 'w2': _w((lambda *s: torch.randn(*s, dtype=torch.bfloat16) * 0.8)(D, D) * D ** (-0.5)), 'w3': _w((lambda *s: torch.randn(*s, dtype=torch.bfloat16) * 0.8)(D, F) * D ** (-0.5)), 'w4': _w((lambda *s: torch.randn(*s, dtype=torch.bfloat16) * 0.8)(F, D) * F ** (-0.5))}
    _B_in = _B_setup()
    return {'A_x': _A_in['x'], 'A_mem0': _A_in['mem0'], 'A_Wq': _A_in['Wq'], 'A_Wk': _A_in['Wk'], 'A_Wv': _A_in['Wv'], 'A_Wo': _A_in['Wo'], 'A_seg_len': _A_in['seg_len'], 'B_x': _B_in['x'], 'B_w1': _B_in['w1'], 'B_w2': _B_in['w2'], 'B_w3': _B_in['w3'], 'B_w4': _B_in['w4']}

def reference(A_x, A_mem0, A_Wq, A_Wk, A_Wv, A_Wo, A_seg_len, B_x, B_w1, B_w2, B_w3, B_w4):

    def _A_ref(x, mem0, Wq, Wk, Wv, Wo, seg_len):
        B, S, Dm = x.shape
        H, Dh = (Wq.shape[1], Wq.shape[2])
        M = mem0.shape[1]
        scale = Dh ** (-0.5)
        n_seg = S // seg_len
        L = M + seg_len
        idx = torch.arange(L)
        mask = (idx[:, None] < M) | (idx[:, None] >= idx[None, :])
        mem = mem0
        outs = []
        for s in range(n_seg):
            xs = x[:, s * seg_len:(s + 1) * seg_len, :].clone()
            h = torch.concatenate([mem, xs], axis=1)
            q = torch.einsum('btd,dhe->bhte', h, Wq)
            k = torch.einsum('btd,dhe->bhte', h, Wk)
            v = torch.einsum('btd,dhe->bhte', h, Wv)
            scores = torch.einsum('bhqe,bhke->bhqk', q, k) * scale
            scores = torch.where(mask[None, None], scores, -1000000000.0)
            p = _softmax(scores, axis=-1)
            o = torch.einsum('bhqk,bhke->bhqe', p, v)
            o = torch.einsum('bhte,hed->btd', o, Wo)
            mem = o[:, :M, :].clone()
            outs.append(o[:, M:, :])
        return torch.concatenate(outs, axis=1)

    def _B_ref(x, w1, w2, w3, w4):
        B, S, D = x.shape
        h = _gelu(_layernorm(x, axis=-1) @ w1)
        c = D // 4
        g0 = _pad(h[:, :, 0 * c:1 * c], ((0, 0), (1, 0), (0, 0)))[:, :S, :]
        g1 = _pad(h[:, :, 1 * c:2 * c], ((0, 0), (0, 1), (0, 0)))[:, 1:, :]
        g2 = _pad(h[:, :, 2 * c:3 * c], ((0, 0), (2, 0), (0, 0)))[:, :S, :]
        g3 = _pad(h[:, :, 3 * c:], ((0, 0), (0, 2), (0, 0)))[:, 2:, :]
        hs = torch.concatenate([g0, g1, g2, g3], axis=-1) @ w2
        x1 = x + hs
        h2 = _gelu(_layernorm(x1, axis=-1) @ w3) @ w4
        return x1 + h2
    x = A_x
    h = x + _A_ref(_layernorm(x, axis=-1), A_mem0, A_Wq, A_Wk, A_Wv, A_Wo, A_seg_len)
    h2 = h + _B_ref(_layernorm(h, axis=-1), B_w1, B_w2, B_w3, B_w4)
    return h2
```

```cpp
#include <hip/hip_runtime.h>
#include <hip/hip_bf16.h>
#include <stdint.h>

typedef __bf16 bf16x2 __attribute__((ext_vector_type(2)));
typedef __bf16 bf16x4 __attribute__((ext_vector_type(4)));
typedef __bf16 bf16x8 __attribute__((ext_vector_type(8)));
typedef __bf16 bf16x16 __attribute__((ext_vector_type(16)));
typedef short s16x4 __attribute__((ext_vector_type(4)));
typedef float f32x2 __attribute__((ext_vector_type(2)));
typedef float f32x4 __attribute__((ext_vector_type(4)));
typedef unsigned u32x2 __attribute__((ext_vector_type(2)));
typedef unsigned u32x4 __attribute__((ext_vector_type(4)));
typedef unsigned u32x8 __attribute__((ext_vector_type(8)));
typedef __attribute__((address_space(3))) s16x4* lds_s16x4_ptr;
typedef __amdgpu_buffer_rsrc_t rsrc_t;

#define DEVI __device__ __forceinline__
#define MEMBAR() asm volatile("" ::: "memory")
#define WAIT_VM0() asm volatile("s_waitcnt vmcnt(0)" ::: "memory")

constexpr int NSEG = 64;
constexpr int SC1 = 16;
constexpr unsigned SPIN_CAP = 400000u;
constexpr unsigned POISON = 0xFFFFFFFFu;

constexpr int MB = 1 << 20;
constexpr int OFF_HO = 0;
constexpr int OFF_FLAG = 256 * 1024;
constexpr int OFF_XNF = 1 * MB;
constexpr int OFF_HF = 3 * MB;
constexpr int OFF_YF = 5 * MB;
constexpr int OFF_H1F = 7 * MB;
constexpr int OFF_X1F = 9 * MB;
constexpr int OFF_AF = 11 * MB;
constexpr int OFF_PX = 18 * MB;
constexpr int OFF_PM = 22 * MB;
constexpr int OFF_BIMG = 24 * MB;
constexpr int OFF_QX = 40 * MB;
constexpr int OFF_KX = 56 * MB;
constexpr int OFF_UM = 72 * MB;
constexpr int OFF_UX = 88 * MB;
constexpr int OFF_DBG = 152 * MB;
constexpr int WS_SPAN = 160 * MB;
constexpr int FL_SEL = 0;
constexpr int FL_ERR = 32;
constexpr int FL_QK = 64;
constexpr int FL_VU = 320;
constexpr int FL_P = 576;
constexpr int FL_U = 640;
constexpr int FL_H = 704;
constexpr int FL_M1 = 768;
constexpr int FL_M2 = 832;
constexpr int FL_M3 = 896;
constexpr int FL_WORDS = 1152;

constexpr int LDS_BYTES = 151552;

struct Params {
    const __bf16* x; const __bf16* mem0; const __bf16* wq; const __bf16* wk; const __bf16* wv; const __bf16* wo;
    const __bf16* w1; const __bf16* w2; const __bf16* w3; const __bf16* w4;
    __bf16* out; char* ws;
};

DEVI unsigned pk2(float a, float b) {
    f32x2 v = {a, b};
    bf16x2 r = __builtin_convertvector(v, bf16x2);
    return __builtin_bit_cast(unsigned, r);
}
DEVI float bflo(unsigned u) { return __builtin_bit_cast(float, u << 16); }
DEVI float bfhi(unsigned u) { return __builtin_bit_cast(float, u & 0xFFFF0000u); }
DEVI float rbf(float a) { return bflo(pk2(a, 0.f)); }
DEVI f32x4 mfma32(u32x4 a, u32x4 b, f32x4 c) {
    return __builtin_amdgcn_mfma_f32_16x16x32_bf16(__builtin_bit_cast(bf16x8, a), __builtin_bit_cast(bf16x8, b), c, 0, 0, 0);
}
DEVI f32x4 mfma16(u32x2 a, u32x2 b, f32x4 c) {
    return __builtin_amdgcn_mfma_f32_16x16x16bf16_1k(__builtin_bit_cast(s16x4, a), __builtin_bit_cast(s16x4, b), c, 0, 0, 0);
}
DEVI f32x4 smfmac(u32x4 a, u32x4 b0, u32x4 b1, f32x4 c, int idx) {
    u32x8 b = __builtin_shufflevector(b0, b1, 0, 1, 2, 3, 4, 5, 6, 7);
    return __builtin_amdgcn_smfmac_f32_16x16x64_bf16(__builtin_bit_cast(bf16x8, a), __builtin_bit_cast(bf16x16, b), c, idx, 0, 0);
}
DEVI u32x4 ld128(rsrc_t rs, int off) { return __builtin_amdgcn_raw_buffer_load_b128(rs, off, 0, SC1); }
DEVI u32x2 ld64(rsrc_t rs, int off) { return __builtin_amdgcn_raw_buffer_load_b64(rs, off, 0, SC1); }
DEVI unsigned ld32(rsrc_t rs, int off) { return __builtin_amdgcn_raw_buffer_load_b32(rs, off, 0, SC1); }
DEVI void st128(rsrc_t rs, int off, u32x4 v) { __builtin_amdgcn_raw_buffer_store_b128(v, rs, off, 0, SC1); }
DEVI void st64(rsrc_t rs, int off, u32x2 v) { __builtin_amdgcn_raw_buffer_store_b64(v, rs, off, 0, SC1); }
DEVI void st32(rsrc_t rs, int off, unsigned v) { __builtin_amdgcn_raw_buffer_store_b32(v, rs, off, 0, SC1); }
DEVI int amap(int x) { return (x & 8191) | ((x >> 13) << 14); }
DEVI unsigned sanit(unsigned u) { return u < 0xFFFFFFFEu ? u : 0xFFFFFFFEu; }

template <int CTRL> DEVI float dppf(float v) {
    return __builtin_bit_cast(float, __builtin_amdgcn_update_dpp(0, __builtin_bit_cast(int, v), CTRL, 0xF, 0xF, true));
}
template <int CTRL> DEVI unsigned dppu(unsigned v) {
    return (unsigned)__builtin_amdgcn_update_dpp(0, (int)v, CTRL, 0xF, 0xF, true);
}
constexpr int DPP_ROR1 = 0x121, DPP_ROR2 = 0x122, DPP_ROR4 = 0x124, DPP_ROR8 = 0x128;
constexpr int DPP_SHL2 = 0x102, DPP_SHL4 = 0x104, DPP_SHL6 = 0x106;
constexpr int DPP_XOR1 = 0xB1;

DEVI void swap32(unsigned& a, unsigned& b) { auto r = __builtin_amdgcn_permlane32_swap(a, b, false, false); a = r[0]; b = r[1]; }
DEVI void swap16(unsigned& a, unsigned& b) { auto r = __builtin_amdgcn_permlane16_swap(a, b, false, false); a = r[0]; b = r[1]; }
DEVI float fold32(float x, float y) {
    unsigned a = __builtin_bit_cast(unsigned, x), b = __builtin_bit_cast(unsigned, y);
    swap32(a, b);
    return __builtin_bit_cast(float, a) + __builtin_bit_cast(float, b);
}
DEVI float xg_sum(float v) {
    unsigned a = __builtin_bit_cast(unsigned, v), b = a; swap16(a, b);
    v = __builtin_bit_cast(float, a) + __builtin_bit_cast(float, b);
    a = __builtin_bit_cast(unsigned, v); b = a; swap32(a, b);
    return __builtin_bit_cast(float, a) + __builtin_bit_cast(float, b);
}
DEVI float xg_max(float v) {
    unsigned a = __builtin_bit_cast(unsigned, v), b = a; swap16(a, b);
    v = fmaxf(__builtin_bit_cast(float, a), __builtin_bit_cast(float, b));
    a = __builtin_bit_cast(unsigned, v); b = a; swap32(a, b);
    return fmaxf(__builtin_bit_cast(float, a), __builtin_bit_cast(float, b));
}
DEVI float wave_sum(float v) {
    v += dppf<DPP_ROR1>(v); v += dppf<DPP_ROR2>(v); v += dppf<DPP_ROR4>(v); v += dppf<DPP_ROR8>(v);
    return xg_sum(v);
}
DEVI float wave_max(float v) {
    v = fmaxf(v, dppf<DPP_ROR1>(v)); v = fmaxf(v, dppf<DPP_ROR2>(v)); v = fmaxf(v, dppf<DPP_ROR4>(v)); v = fmaxf(v, dppf<DPP_ROR8>(v));
    return xg_max(v);
}
DEVI float gelu_t(float x) {
    const float u = x * (0.7978845608028654f + 0.0356774081363001f * x * x);
    const float e = __builtin_amdgcn_exp2f(-2.885390081777927f * u);
    return x * __builtin_amdgcn_rcpf(1.0f + e);
}

DEVI void wait_flags(rsrc_t rs, int off, bool act, unsigned v, bool& dead, int slp) {
    if (dead) return;
    for (unsigned sp = 0;; ++sp) {
        unsigned f = ld32(rs, off);
        MEMBAR();
        if (__all(!act || f >= v)) break;
        if (sp > SPIN_CAP) { dead = true; st32(rs, OFF_FLAG + FL_ERR * 4, 1u); break; }
        for (int i = 0; i < slp; ++i) __builtin_amdgcn_s_sleep(2);
    }
}

template <int NROWS, int NCOLS, bool PERM>
DEVI void stage_rows(const __bf16* __restrict__ src, int ld, char* lds, int tid) {
    constexpr int CPR = NCOLS / 8;
    constexpr int TOT = NROWS * CPR;
    constexpr int PER = TOT / 512;
    constexpr int PITCH = NCOLS * 2 + 32;
    u32x4 v[PER];
#pragma unroll
    for (int i = 0; i < PER; ++i) {
        const int c = tid + 512 * i, row = c / CPR, cc = c % CPR;
        v[i] = *(const u32x4*)(src + (size_t)row * ld + cc * 8);
    }
#pragma unroll
    for (int i = 0; i < PER; ++i) {
        const int c = tid + 512 * i, row = c / CPR, cc = c % CPR;
        int lrow = row;
        if (PERM) {
            const int d = row & 31;
            const int g = 2 * (d >> 4) + ((d >> 2) & 1), r = 2 * ((d >> 3) & 1) + (d & 1), hf = (d >> 1) & 1;
            lrow = (row & ~31) + 16 * hf + 4 * g + r;
        }
        *(u32x4*)(lds + lrow * PITCH + cc * 16) = v[i];
    }
}
template <int NCOLS>
DEVI u32x4 tr_frag(const char* lds, int ks, int col0, int lane) {
    constexpr int PITCH = NCOLS * 2 + 32;
    const int g = lane >> 4, i = lane & 15;
    const char* p = lds + (32 * ks + 4 * g + (i >> 2)) * PITCH + (col0 + 4 * (i & 3)) * 2;
    s16x4 a = __builtin_amdgcn_ds_read_tr16_b64_v4i16((lds_s16x4_ptr)p);
    s16x4 b = __builtin_amdgcn_ds_read_tr16_b64_v4i16((lds_s16x4_ptr)(p + 16 * PITCH));
    u32x2 ua = __builtin_bit_cast(u32x2, a), ub = __builtin_bit_cast(u32x2, b);
    return u32x4{ua.x, ua.y, ub.x, ub.y};
}

__global__ void __launch_bounds__(512, 2) prep_kernel(Params p) {
    extern __shared__ __attribute__((aligned(16))) char lds[];
    const int tid = threadIdx.x, lane = tid & 63, w = tid >> 6, b = blockIdx.x;
    const int h = b & 31, t = b >> 5;
    char* ws = p.ws;
    if (b < 32) {
        const int phys = (b * 512 + tid) * 16;
        u32x4 v = {POISON, POISON, POISON, POISON};
        if (phys < 16384) {
            const int o = phys & 8191, j = o >> 7, r = (o >> 4) & 7;
            v = *(const u32x4*)(p.mem0 + r * 512 + 8 * j);
            v.x = sanit(v.x); v.y = sanit(v.y); v.z = sanit(v.z); v.w = sanit(v.w);
        }
        *(u32x4*)(ws + OFF_HO + phys) = v;
    } else if (b < 40) {
        const int i = (b - 32) * 512 + tid;
        if (i < FL_WORDS) *(unsigned*)(ws + OFF_FLAG + i * 4) = 0u;
    }
    stage_rows<128, 512, false>(p.wo + (size_t)h * 128 * 512, 512, lds, tid);
    u32x4 av[4][4];
    {
        const int g = lane >> 4;
#pragma unroll
        for (int dt = 0; dt < 4; ++dt)
#pragma unroll
            for (int ks = 0; ks < 4; ++ks) {
                const __bf16* src = p.wv + (size_t)(64 * t + 16 * dt + (lane & 15)) * 4096 + h * 128 + 32 * ks + 4 * g;
                u32x2 lo = *(const u32x2*)src, hi = *(const u32x2*)(src + 16);
                av[dt][ks] = u32x4{lo.x, lo.y, hi.x, hi.y};
            }
    }
    {
        const int T = 8 * b + w, ks = lane >> 2, g = lane & 3;
        const __bf16* src = p.x + (size_t)T * 512 + 32 * ks + 4 * g;
        u32x2 lo = *(const u32x2*)src, hi = *(const u32x2*)(src + 16);
        float f[8] = {bflo(lo.x), bfhi(lo.x), bflo(lo.y), bfhi(lo.y), bflo(hi.x), bfhi(hi.x), bflo(hi.y), bfhi(hi.y)};
        float s = 0.f;
#pragma unroll
        for (int i = 0; i < 8; ++i) s += f[i];
        const float mean = wave_sum(s) * (1.0f / 512.0f);
        float q = 0.f;
#pragma unroll
        for (int i = 0; i < 8; ++i) { f[i] -= mean; q += f[i] * f[i]; }
        const float rstd = __builtin_amdgcn_rsqf(wave_sum(q) * (1.0f / 512.0f) + 1e-5f);
        u32x4 o = {pk2(f[0] * rstd, f[1] * rstd), pk2(f[2] * rstd, f[3] * rstd), pk2(f[4] * rstd, f[5] * rstd), pk2(f[6] * rstd, f[7] * rstd)};
        *(u32x4*)(ws + OFF_XNF + ((T >> 4) * 16 + ks) * 1024 + (16 * g + (T & 15)) * 16) = o;
    }
    __syncthreads();
#pragma unroll
    for (int ci = 0; ci < 4; ++ci) {
        const int ct = 4 * w + ci;
        u32x4 bw[4];
#pragma unroll
        for (int ks = 0; ks < 4; ++ks) bw[ks] = tr_frag<512>(lds, ks, 16 * ct, lane);
        f32x4 acc[4];
#pragma unroll
        for (int dt = 0; dt < 4; ++dt) {
            acc[dt] = f32x4{0.f, 0.f, 0.f, 0.f};
#pragma unroll
            for (int ks = 0; ks < 4; ++ks) acc[dt] = mfma32(av[dt][ks], bw[ks], acc[dt]);
        }
#pragma unroll
        for (int half = 0; half < 2; ++half) {
            unsigned x0 = pk2(acc[2 * half][0], acc[2 * half][1]), x1 = pk2(acc[2 * half][2], acc[2 * half][3]);
            unsigned y0 = pk2(acc[2 * half + 1][0], acc[2 * half + 1][1]), y1 = pk2(acc[2 * half + 1][2], acc[2 * half + 1][3]);
            swap32(x0, y0); swap32(x1, y1);
            const int c16 = lane & 15, bb = (lane >> 4) & 1, tile = lane >> 5;
            const int gB = 2 * tile + bb, j = 2 * ct + (c16 >> 3), u = c16 & 7;
            const int n = 8 * (h & 1) + u;
            const size_t off = (((((size_t)j * 8 + (h >> 2)) * 2 + ((h >> 1) & 1)) * 8 + t) * 2 + half) * 1024 + (16 * gB + n) * 16;
            *(u32x4*)(ws + OFF_BIMG + off) = u32x4{x0, y0, x1, y1};
        }
    }
}

DEVI void role_probe(rsrc_t rs, char* lds, int tid) {
    const int lane = tid & 63, w = tid >> 6;
    unsigned* t = (unsigned*)lds;
    if (w < 2) {
        unsigned best = 0xFFFFFFFFu, acc = 0;
        for (int rep = 0; rep < 5; ++rep) {
            unsigned long long c0 = __builtin_amdgcn_s_memtime();
            unsigned v = ld32(rs, OFF_HO + 0x30000 + w * 8192 + rep * 256 + (acc & 0));
            asm volatile("s_waitcnt vmcnt(0)" ::"v"(v) : "memory");
            unsigned long long c1 = __builtin_amdgcn_s_memtime();
            v = ld32(rs, OFF_HO + 0x30000 + w * 8192 + rep * 256 + (v & 0));
            asm volatile("s_waitcnt vmcnt(0)" ::"v"(v) : "memory");
            unsigned long long c2 = __builtin_amdgcn_s_memtime();
            acc += v;
            const unsigned d = (unsigned)(c2 - c1);
            best = d < best ? d : best;
            (void)c0;
        }
        if (lane == 0) t[w] = best + (acc & 0);
    }
    __syncthreads();
    if (tid == 0) {
        const unsigned xcc = __builtin_amdgcn_s_getreg((20 << 0) | (0 << 6) | (3 << 11)) & 15u;
        unsigned near1 = t[1] < t[0] ? 1u : 0u;
        if (xcc >= 4) near1 ^= 1u;
        st32(rs, OFF_FLAG + FL_SEL * 4, 1u + near1);
    }
}

DEVI int get_base(rsrc_t rs, bool& dead) {
    unsigned f = 0;
    for (unsigned sp = 0;; ++sp) {
        f = ld32(rs, OFF_FLAG + FL_SEL * 4);
        MEMBAR();
        f = __builtin_amdgcn_readfirstlane(f);
        if (f != 0) break;
        if (sp > SPIN_CAP) { dead = true; st32(rs, OFF_FLAG + FL_ERR * 4, 2u); break; }
        __builtin_amdgcn_s_sleep(4);
    }
    return f == 2 ? 8192 : 0;
}
DEVI bool okw(u32x4 v) { return (v.x != POISON) & (v.y != POISON) & (v.z != POISON) & (v.w != POISON); }
DEVI bool okw2(u32x2 v) { return (v.x != POISON) & (v.y != POISON); }
constexpr int HO_MEM = 0, HO_P = 32768, HO_PSTR = 24576;

#define SLP_S 0
#define SLP_C1 0
#define SLP_C2 0
DEVI void nap(int n) { for (int i = 0; i < n; ++i) __builtin_amdgcn_s_sleep(1); }

DEVI void role_score(const Params& p, rsrc_t rs, char* lds, int tid, int h) {
    const int lane = tid & 63, w = tid >> 6, g = lane >> 4, l16 = lane & 15;
    u32x4 wq[16], wk[16];
    stage_rows<512, 128, true>(p.wq + h * 128, 4096, lds, tid);
    __syncthreads();
#pragma unroll
    for (int ks = 0; ks < 16; ++ks) wq[ks] = tr_frag<128>(lds, ks, 16 * w, lane);
    __syncthreads();
    stage_rows<512, 128, true>(p.wk + h * 128, 4096, lds, tid);
    __syncthreads();
#pragma unroll
    for (int ks = 0; ks < 16; ++ks) wk[ks] = tr_frag<128>(lds, ks, 16 * w, lane);
    __syncthreads();
    char* membuf = lds;
    char* kbuf = lds + 8192;
    char* qbuf = lds + 8192 + 16384;
    char* ybuf = lds + 8192 + 32768;
    float* sbuf = (float*)(lds + 8192 + 32768 + 8192);
    const int idx = l16 < 8 ? 0x4444 : 0xEEEE;
    u32x2 ident;
    {
        unsigned e[4];
#pragma unroll
        for (int j = 0; j < 4; ++j) e[j] = (4 * g + j == l16) ? 0x3F80u : 0u;
        ident = u32x2{e[0] | (e[1] << 16), e[2] | (e[3] << 16)};
    }
    bool dead = false;
    const int base = get_base(rs, dead);
    const int fqk = OFF_FLAG + (FL_QK + h * 8 + w) * 4;
    wait_flags(rs, fqk, true, 1u, dead, 2);
    u32x4 kx = ld128(rs, OFF_KX + (h * 8 + w) * 1024 + lane * 16);
    u32x4 qx = ld128(rs, OFF_QX + (h * 8 + w) * 1024 + lane * 16);
    const float sc2 = 0.08838834764831845f * 1.4426950408889634f;
    for (int s = 0; s < NSEG; ++s) {
        const int par = s & 1;
        u32x4 v;
        {
            const int off = OFF_HO + base + amap(HO_MEM + (s & 3) * 8192 + w * 1024 + lane * 16);
            for (unsigned sp = 0;; ++sp) {
                v = ld128(rs, off);
                MEMBAR();
                if (__all(okw(v))) break;
                if (dead || sp > SPIN_CAP) { if (!dead) st32(rs, OFF_FLAG + FL_ERR * 4, 3u); dead = true; break; }
            }
        }
        *(u32x4*)(membuf + w * 1024 + lane * 16) = v;
        *(u32x4*)(kbuf + par * 8192 + w * 1024 + lane * 16) = kx;
        *(u32x4*)(qbuf + par * 8192 + w * 1024 + lane * 16) = qx;
        if (s > 0 && w < 2 && lane == 0) st32(rs, OFF_FLAG + (FL_P + h * 2 + w) * 4, (unsigned)s);
        __syncthreads();
        f32x4 aq = {0.f, 0.f, 0.f, 0.f}, ak = {0.f, 0.f, 0.f, 0.f};
#pragma unroll
        for (int t = 0; t < 8; ++t) {
            const u32x4 a = *(const u32x4*)(membuf + t * 1024 + lane * 16);
            aq = smfmac(a, wq[2 * t], wq[2 * t + 1], aq, idx);
            ak = smfmac(a, wk[2 * t], wk[2 * t + 1], ak, idx);
        }
        f32x4 X;
#pragma unroll
        for (int i = 0; i < 4; ++i) {
            X[i] = fold32(aq[i], ak[i]);
        }
        const u32x2 xb = {pk2(X[0], X[1]), pk2(X[2], X[3])};
        const f32x4 yt = mfma16(xb, ident, f32x4{0.f, 0.f, 0.f, 0.f});
        const u32x2 y = {pk2(yt[0], yt[1]), pk2(yt[2], yt[3])};
        *(u32x2*)(ybuf + par * 4096 + w * 512 + lane * 8) = y;
        if (s < NSEG - 1) {
            const f32x4 z4 = {0.f, 0.f, 0.f, 0.f};
            const f32x4 s0 = mfma16(u32x2{kx.x, kx.y}, y, z4);
            const f32x4 s1 = mfma16(u32x2{kx.z, kx.w}, y, z4);
            const f32x4 sm = mfma16(y, y, z4);
            if (l16 < 8) {
                float* d = sbuf + (w * 8 + l16) * 44;
                *(f32x4*)(d + 8 * g) = s0;
                *(f32x4*)(d + 8 * g + 4) = s1;
                if (g >= 2) *(f32x4*)(d + 32 + 4 * (g - 2)) = sm;
            }
        }
        __syncthreads();
        if (s < NSEG - 1) {
            float sv = 0.f;
            const int ln = lane < 40 ? lane : 0;
#pragma unroll
            for (int i = 0; i < 8; ++i) sv += sbuf[(i * 8 + w) * 44 + ln];
            sv = lane < 40 ? sv * sc2 : -INFINITY;
            const float mx = wave_max(sv);
            const float e = __builtin_amdgcn_exp2f(sv - mx);
            const float sum = wave_sum(e);
            const float pr = e * __builtin_amdgcn_rcpf(sum);
            const unsigned d0 = sanit(pk2(pr, dppf<DPP_XOR1>(pr)));
            const u32x4 o = {d0, dppu<DPP_SHL2>(d0), dppu<DPP_SHL4>(d0), dppu<DPP_SHL6>(d0)};
            const int lo = lane < 32 ? h * 512 + w * 64 + (lane >> 3) * 16 : 16384 + h * 128 + w * 16;
            if ((lane & 7) == 0 && lane <= 32) {
                st128(rs, OFF_HO + base + amap(HO_P + (s & 3) * HO_PSTR + lo), o);
                st128(rs, OFF_HO + base + amap(HO_P + ((s + 2) & 3) * HO_PSTR + lo), u32x4{POISON, POISON, POISON, POISON});
            }
            wait_flags(rs, fqk, true, (unsigned)(s + 2), dead, 2);
            kx = ld128(rs, OFF_KX + (((s + 1) * 32 + h) * 8 + w) * 1024 + lane * 16);
            qx = ld128(rs, OFF_QX + (((s + 1) * 32 + h) * 8 + w) * 1024 + lane * 16);
        }
        if (w < 2) {
            f32x4 dm = {0.f, 0.f, 0.f, 0.f}, d0 = dm, d1 = dm;
#pragma unroll
            for (int t = 0; t < 8; ++t) {
                const u32x4 kf = *(const u32x4*)(kbuf + par * 8192 + t * 1024 + lane * 16);
                const u32x2 qf = *(const u32x2*)(qbuf + par * 8192 + t * 1024 + lane * 16 + w * 8);
                const u32x2 yf = *(const u32x2*)(ybuf + par * 4096 + t * 512 + lane * 8);
                dm = mfma16(yf, qf, dm);
                d0 = mfma16(u32x2{kf.x, kf.y}, qf, d0);
                d1 = mfma16(u32x2{kf.z, kf.w}, qf, d1);
            }
            const int tok = 16 * w + l16;
            float mx = -INFINITY;
#pragma unroll
            for (int i = 0; i < 4; ++i) {
                dm[i] = g >= 2 ? dm[i] * sc2 : -INFINITY;
                d0[i] = (4 * g + i <= tok) ? d0[i] * sc2 : -INFINITY;
                d1[i] = (16 + 4 * g + i <= tok) ? d1[i] * sc2 : -INFINITY;
                mx = fmaxf(mx, fmaxf(dm[i], fmaxf(d0[i], d1[i])));
            }
            mx = xg_max(mx);
            float sum = 0.f;
#pragma unroll
            for (int i = 0; i < 4; ++i) {
                dm[i] = __builtin_amdgcn_exp2f(dm[i] - mx); d0[i] = __builtin_amdgcn_exp2f(d0[i] - mx); d1[i] = __builtin_amdgcn_exp2f(d1[i] - mx);
                sum += dm[i] + d0[i] + d1[i];
            }
            const float r = __builtin_amdgcn_rcpf(xg_sum(sum));
            st128(rs, OFF_PX + ((s * 32 + h) * 2 + w) * 1024 + lane * 16,
                  u32x4{pk2(d0[0] * r, d0[1] * r), pk2(d0[2] * r, d0[3] * r), pk2(d1[0] * r, d1[1] * r), pk2(d1[2] * r, d1[3] * r)});
            if (g >= 2) st64(rs, OFF_PM + (s * 32 + h) * 512 + tok * 16 + (g - 2) * 8, u32x2{pk2(dm[0] * r, dm[1] * r), pk2(dm[2] * r, dm[3] * r)});
            if (s == NSEG - 1) { WAIT_VM0(); if (lane == 0) st32(rs, OFF_FLAG + (FL_P + h * 2 + w) * 4, (unsigned)NSEG); }
        }
        nap(SLP_S);
    }
}

DEVI void role_col(const Params& p, rsrc_t rs, char* lds, int tid, int j) {
    const int lane = tid & 63, w = tid >> 6, g = lane >> 4, l16 = lane & 15;
    u32x4 bi[2][16];
    {
        const char* src = p.ws + OFF_BIMG + (size_t)(j * 8 + w) * 32768 + lane * 16;
#pragma unroll
        for (int ii = 0; ii < 2; ++ii)
#pragma unroll
            for (int k = 0; k < 16; ++k) bi[ii][k] = *(const u32x4*)(src + (ii * 16 + k) * 1024);
    }
    char* membuf = lds;
    float* red = (float*)(lds + 8192);
    const int idx = l16 < 8 ? 0x4444 : 0xEEEE;
    bool dead = false;
    const int base = get_base(rs, dead);
    const int fvu = OFF_FLAG + (FL_VU + (j >> 3) * 32 + 4 * w + (lane & 3)) * 4;
    const int uoff = ((4 * w + (l16 >> 3)) * 32 + (j >> 1)) * 1024 + (g * 16 + 8 * (j & 1) + (l16 & 7)) * 16;
    wait_flags(rs, fvu, true, 1u, dead, 2);
    u32x4 ux0 = ld128(rs, OFF_UX + uoff), ux1 = ld128(rs, OFF_UX + uoff + 2 * 32768);
    for (int s = 0; s < NSEG; ++s) {
        u32x4 v;
        {
            const int off = OFF_HO + base + amap(HO_MEM + (s & 3) * 8192 + w * 1024 + lane * 16);
            for (unsigned sp = 0;; ++sp) {
                v = ld128(rs, off);
                MEMBAR();
                if (__all(okw(v))) break;
                if (dead || sp > SPIN_CAP) { if (!dead) st32(rs, OFF_FLAG + FL_ERR * 4, 4u); dead = true; break; }
            }
        }
        *(u32x4*)(membuf + w * 1024 + lane * 16) = v;
        __syncthreads();
        u32x4 un0 = ux0, un1 = ux1;
        if (s + 1 < NSEG - 1) {
            wait_flags(rs, fvu, true, (unsigned)(s + 2), dead, 2);
            un0 = ld128(rs, OFF_UX + (s + 1) * MB + uoff);
            un1 = ld128(rs, OFF_UX + (s + 1) * MB + uoff + 2 * 32768);
        }
        f32x4 a0 = {0.f, 0.f, 0.f, 0.f}, a1 = a0;
#pragma unroll
        for (int t = 0; t < 8; ++t) {
            const u32x4 a = *(const u32x4*)(membuf + t * 1024 + lane * 16);
            a0 = smfmac(a, bi[0][2 * t], bi[0][2 * t + 1], a0, idx);
            a1 = smfmac(a, bi[1][2 * t], bi[1][2 * t + 1], a1, idx);
        }
        f32x4 X;
#pragma unroll
        for (int i = 0; i < 4; ++i) {
            X[i] = fold32(a0[i], a1[i]);
        }
        const u32x2 xb = {pk2(X[0], X[1]), pk2(X[2], X[3])};
        st64(rs, OFF_UM + s * (256 * 1024) + ((4 * w + 2 * (g >> 1) + (l16 >> 3)) * 512 + 8 * j + (l16 & 7)) * 16 + (g & 1) * 8, xb);
        if (s == NSEG - 1) {
            WAIT_VM0();
            __syncthreads();
            if (tid == 0) st32(rs, OFF_FLAG + (FL_U + j) * 4, (unsigned)NSEG);
            break;
        }
        nap(SLP_C1);
        u32x4 p0, p1; u32x2 pm;
        {
            const int pb = HO_P + (s & 3) * HO_PSTR;
            const int o0 = OFF_HO + base + amap(pb + (4 * w + (l16 >> 3)) * 512 + (l16 & 7) * 64 + g * 16);
            const int o1 = OFF_HO + base + amap(pb + (4 * w + 2 + (l16 >> 3)) * 512 + (l16 & 7) * 64 + g * 16);
            const int om = OFF_HO + base + amap(pb + 16384 + (4 * w + 2 * (g >> 1) + (l16 >> 3)) * 128 + (l16 & 7) * 16 + (g & 1) * 8);
            for (unsigned sp = 0;; ++sp) {
                p0 = ld128(rs, o0); p1 = ld128(rs, o1); pm = ld64(rs, om);
                MEMBAR();
                if (__all(okw(p0) & okw(p1) & okw2(pm))) break;
                if (dead || sp > SPIN_CAP) { if (!dead) st32(rs, OFF_FLAG + FL_ERR * 4, 5u); dead = true; break; }
            }
        }
        f32x4 D = mfma32(p0, ux0, f32x4{0.f, 0.f, 0.f, 0.f});
        const f32x4 Dm = mfma16(pm, xb, f32x4{0.f, 0.f, 0.f, 0.f});
        D = mfma32(p1, ux1, D);
        D += Dm;
        if ((l16 < 8) == (g < 2)) {
            const int blk = g >> 1;
            *(f32x4*)(red + (w * 2 + blk) * 64 + (l16 & 7) * 8 + 4 * (g & 1)) = D;
        }
        WAIT_VM0();
        __syncthreads();
        if (w == 0) {
            const int r = lane >> 3, u = lane & 7;
            float sv = 0.f;
#pragma unroll
            for (int i = 0; i < 16; ++i) sv += red[i * 64 + u * 8 + r];
            const unsigned d0 = sanit(pk2(sv, dppf<DPP_XOR1>(sv)));
            const u32x4 o = {d0, dppu<DPP_SHL2>(d0), dppu<DPP_SHL4>(d0), dppu<DPP_SHL6>(d0)};
            if (u == 0) {
                st128(rs, OFF_HO + base + amap(HO_MEM + ((s + 1) & 3) * 8192 + j * 128 + r * 16), o);
                st128(rs, OFF_HO + base + amap(HO_MEM + ((s + 3) & 3) * 8192 + j * 128 + r * 16), u32x4{POISON, POISON, POISON, POISON});
            }
            if (lane == 0) st32(rs, OFF_FLAG + (FL_U + j) * 4, (unsigned)(s + 1));
        }
        ux0 = un0; ux1 = un1;
        nap(SLP_C2);
    }
}

DEVI void role_qk(const Params& p, rsrc_t rs, char* lds, int tid, int h) {
    const int lane = tid & 63, w = tid >> 6;
    u32x4 wq[16], wk[16];
    stage_rows<512, 128, false>(p.wq + h * 128, 4096, lds, tid);
    __syncthreads();
#pragma unroll
    for (int ks = 0; ks < 16; ++ks) wq[ks] = tr_frag<128>(lds, ks, 16 * w, lane);
    __syncthreads();
    stage_rows<512, 128, false>(p.wk + h * 128, 4096, lds, tid);
    __syncthreads();
#pragma unroll
    for (int ks = 0; ks < 16; ++ks) wk[ks] = tr_frag<128>(lds, ks, 16 * w, lane);
    __syncthreads();
    const char* xn = p.ws + OFF_XNF + tid * 16;
    u32x4 xr[4];
#pragma unroll
    for (int i = 0; i < 4; ++i) xr[i] = *(const u32x4*)(xn + i * 8192);
    const int fo = OFF_FLAG + (FL_QK + h * 8 + w) * 4;
    for (int s = 0; s < NSEG; ++s) {
        char* xb = lds + (s & 1) * 32768;
#pragma unroll
        for (int i = 0; i < 4; ++i) *(u32x4*)(xb + i * 8192 + tid * 16) = xr[i];
        __syncthreads();
        if (s + 1 < NSEG) {
#pragma unroll
            for (int i = 0; i < 4; ++i) xr[i] = *(const u32x4*)(xn + (s + 1) * 32768 + i * 8192);
        }
        f32x4 q0 = {0.f, 0.f, 0.f, 0.f}, q1 = q0, k0 = q0, k1 = q0;
#pragma unroll
        for (int ks = 0; ks < 16; ++ks) {
            const u32x4 b0 = *(const u32x4*)(xb + ks * 1024 + lane * 16);
            const u32x4 b1 = *(const u32x4*)(xb + 16384 + ks * 1024 + lane * 16);
            q0 = mfma32(wq[ks], b0, q0); q1 = mfma32(wq[ks], b1, q1);
            k0 = mfma32(wk[ks], b0, k0); k1 = mfma32(wk[ks], b1, k1);
        }
        WAIT_VM0();
        if (s > 0 && lane == 0) st32(rs, fo, (unsigned)s);
        const int o = ((s * 32 + h) * 8 + w) * 1024 + lane * 16;
        st128(rs, OFF_QX + o, u32x4{pk2(q0[0], q0[1]), pk2(q0[2], q0[3]), pk2(q1[0], q1[1]), pk2(q1[2], q1[3])});
        st128(rs, OFF_KX + o, u32x4{pk2(k0[0], k0[1]), pk2(k0[2], k0[3]), pk2(k1[0], k1[1]), pk2(k1[2], k1[3])});
        if (s < 2 || s == NSEG - 1) { WAIT_VM0(); if (lane == 0) st32(rs, fo, (unsigned)(s + 1)); }
    }
}

DEVI void role_vu(const Params& p, rsrc_t rs, char* lds, int tid, int h) {
    const int lane = tid & 63, w = tid >> 6;
    u32x4 wv[16], wo[16];
    stage_rows<512, 128, false>(p.wv + h * 128, 4096, lds, tid);
    __syncthreads();
#pragma unroll
    for (int ks = 0; ks < 16; ++ks) wv[ks] = tr_frag<128>(lds, ks, 16 * w, lane);
    __syncthreads();
    stage_rows<128, 512, false>(p.wo + (size_t)h * 128 * 512, 512, lds, tid);
    __syncthreads();
#pragma unroll
    for (int ci = 0; ci < 4; ++ci)
#pragma unroll
        for (int ks = 0; ks < 4; ++ks) wo[ci * 4 + ks] = tr_frag<512>(lds, ks, 16 * (4 * w + ci), lane);
    __syncthreads();
    const char* xn = p.ws + OFF_XNF + tid * 16;
    u32x4 xr[4];
#pragma unroll
    for (int i = 0; i < 4; ++i) xr[i] = *(const u32x4*)(xn + i * 8192);
    char* vbuf = lds + 65536;
    const int fo = OFF_FLAG + (FL_VU + w * 32 + h) * 4;
    for (int s = 0; s < NSEG; ++s) {
        char* xb = lds + (s & 1) * 32768;
#pragma unroll
        for (int i = 0; i < 4; ++i) *(u32x4*)(xb + i * 8192 + tid * 16) = xr[i];
        __syncthreads();
        if (s + 1 < NSEG) {
#pragma unroll
            for (int i = 0; i < 4; ++i) xr[i] = *(const u32x4*)(xn + (s + 1) * 32768 + i * 8192);
        }
        f32x4 v0 = {0.f, 0.f, 0.f, 0.f}, v1 = v0, v0b = v0, v1b = v0;
#pragma unroll
        for (int ks = 0; ks < 16; ks += 2) {
            const u32x4 b0 = *(const u32x4*)(xb + ks * 1024 + lane * 16);
            const u32x4 b1 = *(const u32x4*)(xb + 16384 + ks * 1024 + lane * 16);
            const u32x4 c0 = *(const u32x4*)(xb + (ks + 1) * 1024 + lane * 16);
            const u32x4 c1 = *(const u32x4*)(xb + 16384 + (ks + 1) * 1024 + lane * 16);
            v0 = mfma32(wv[ks], b0, v0); v1 = mfma32(wv[ks], b1, v1);
            v0b = mfma32(wv[ks + 1], c0, v0b); v1b = mfma32(wv[ks + 1], c1, v1b);
        }
        v0 += v0b; v1 += v1b;
        *(u32x2*)(vbuf + (w >> 1) * 1024 + lane * 16 + (w & 1) * 8) = u32x2{pk2(v0[0], v0[1]), pk2(v0[2], v0[3])};
        *(u32x2*)(vbuf + 4096 + (w >> 1) * 1024 + lane * 16 + (w & 1) * 8) = u32x2{pk2(v1[0], v1[1]), pk2(v1[2], v1[3])};
        __syncthreads();
        u32x4 va[2][4];
#pragma unroll
        for (int mt = 0; mt < 2; ++mt)
#pragma unroll
            for (int ks = 0; ks < 4; ++ks) va[mt][ks] = *(const u32x4*)(vbuf + mt * 4096 + ks * 1024 + lane * 16);
        f32x4 u[4][2];
#pragma unroll
        for (int ci = 0; ci < 4; ++ci)
#pragma unroll
            for (int mt = 0; mt < 2; ++mt) {
                u[ci][mt] = f32x4{0.f, 0.f, 0.f, 0.f};
            }
#pragma unroll
        for (int ks = 0; ks < 4; ++ks)
#pragma unroll
            for (int ci = 0; ci < 4; ++ci)
#pragma unroll
                for (int mt = 0; mt < 2; ++mt) u[ci][mt] = mfma32(va[mt][ks], wo[ci * 4 + ks], u[ci][mt]);
        WAIT_VM0();
        if (s > 0 && lane == 0) st32(rs, fo, (unsigned)s);
#pragma unroll
        for (int ci = 0; ci < 4; ++ci)
            st128(rs, OFF_UX + s * MB + (h * 32 + 4 * w + ci) * 1024 + lane * 16,
                  u32x4{pk2(u[ci][0][0], u[ci][0][1]), pk2(u[ci][0][2], u[ci][0][3]), pk2(u[ci][1][0], u[ci][1][1]), pk2(u[ci][1][2], u[ci][1][3])});
        if (s < 2 || s == NSEG - 1) { WAIT_VM0(); if (lane == 0) st32(rs, fo, (unsigned)(s + 1)); }
    }
}

DEVI void role_tb2(const Params& p, rsrc_t rs, char* lds, int tid, int par, int i) {
    const int lane = tid & 63, w = tid >> 6, g = lane >> 4, l16 = lane & 15;
    f32x4* red = (f32x4*)lds;
    bool dead = false;
    for (int s = par; s < NSEG; s += 2) {
        wait_flags(rs, OFF_FLAG + (FL_VU + (i >> 1) * 32 + 4 * w + (lane & 3)) * 4, true, (unsigned)(s + 1), dead, 4);
        u32x4 ua[4][2];
#pragma unroll
        for (int hh = 0; hh < 4; ++hh)
#pragma unroll
            for (int ct = 0; ct < 2; ++ct) ua[hh][ct] = ld128(rs, OFF_UX + s * MB + ((4 * w + hh) * 32 + 2 * i + ct) * 1024 + lane * 16);
        u32x2 xl = {0u, 0u}, xh = {0u, 0u};
        if (w < 2) {
            const __bf16* xs = p.x + (size_t)(32 * s + 16 * w + l16) * 512 + 32 * i + 4 * g;
            xl = *(const u32x2*)xs; xh = *(const u32x2*)(xs + 16);
        }
        {
            const int o = lane < 8 ? (FL_P + 8 * w + lane) : (FL_U + 4 * i + (lane & 3));
            wait_flags(rs, OFF_FLAG + o * 4, true, (unsigned)(s + 1), dead, 4);
        }
        u32x4 pb[4][2], pm[2], um[2];
#pragma unroll
        for (int hh = 0; hh < 4; ++hh)
#pragma unroll
            for (int mt = 0; mt < 2; ++mt) pb[hh][mt] = ld128(rs, OFF_PX + ((s * 32 + 4 * w + hh) * 2 + mt) * 1024 + lane * 16);
#pragma unroll
        for (int mt = 0; mt < 2; ++mt) pm[mt] = ld128(rs, OFF_PM + (s * 32 + 4 * w + g) * 512 + (16 * mt + l16) * 16);
#pragma unroll
        for (int ct = 0; ct < 2; ++ct) um[ct] = ld128(rs, OFF_UM + s * (256 * 1024) + ((4 * w + g) * 512 + 32 * i + 16 * ct + l16) * 16);
        f32x4 acc[2][2];
#pragma unroll
        for (int ct = 0; ct < 2; ++ct)
#pragma unroll
            for (int mt = 0; mt < 2; ++mt) {
                acc[ct][mt] = mfma32(um[ct], pm[mt], f32x4{0.f, 0.f, 0.f, 0.f});
#pragma unroll
                for (int hh = 0; hh < 4; ++hh) acc[ct][mt] = mfma32(ua[hh][ct], pb[hh][mt], acc[ct][mt]);
            }
        __syncthreads();
#pragma unroll
        for (int ct = 0; ct < 2; ++ct)
#pragma unroll
            for (int mt = 0; mt < 2; ++mt) red[((w * 2 + ct) * 2 + mt) * 64 + lane] = acc[ct][mt];
        __syncthreads();
        if (w < 2) {
            f32x4 s0 = {0.f, 0.f, 0.f, 0.f}, s1 = s0;
#pragma unroll
            for (int k = 0; k < 8; ++k) { s0 += red[((k * 2 + 0) * 2 + w) * 64 + lane]; s1 += red[((k * 2 + 1) * 2 + w) * 64 + lane]; }
            const u32x4 o = {pk2(bflo(xl.x) + s0[0], bfhi(xl.x) + s0[1]), pk2(bflo(xl.y) + s0[2], bfhi(xl.y) + s0[3]),
                             pk2(bflo(xh.x) + s1[0], bfhi(xh.x) + s1[1]), pk2(bflo(xh.y) + s1[2], bfhi(xh.y) + s1[3])};
            st128(rs, OFF_HF + ((2 * s + w) * 16 + i) * 1024 + lane * 16, o);
            WAIT_VM0();
            if (lane == 0) st32(rs, OFF_FLAG + (FL_H + (par * 16 + i) * 2 + w) * 4, (unsigned)(s + 1));
        }
    }
}

DEVI void unpack8(u32x4 v, float* f) {
    f[0] = bflo(v.x); f[1] = bfhi(v.x); f[2] = bflo(v.y); f[3] = bfhi(v.y); f[4] = bflo(v.z); f[5] = bfhi(v.z); f[6] = bflo(v.w); f[7] = bfhi(v.w);
}
DEVI u32x4 pack8(const float* f) { return u32x4{pk2(f[0], f[1]), pk2(f[2], f[3]), pk2(f[4], f[5]), pk2(f[6], f[7])}; }
DEVI void coop_ln(u32x4 (&fr)[2][2], f32x2* st, int lane, int w) {
    float f[2][16];
#pragma unroll
    for (int mt = 0; mt < 2; ++mt) {
        unpack8(fr[mt][0], f[mt]); unpack8(fr[mt][1], f[mt] + 8);
        float a = 0.f, q = 0.f;
#pragma unroll
        for (int i = 0; i < 16; ++i) { a += f[mt][i]; q += f[mt][i] * f[mt][i]; }
        a = xg_sum(a); q = xg_sum(q);
        if (lane < 16) st[(mt * 16 + lane) * 8 + w] = f32x2{a, q};
    }
    __syncthreads();
#pragma unroll
    for (int mt = 0; mt < 2; ++mt) {
        float a = 0.f, q = 0.f;
        const f32x4* sp = (const f32x4*)(st + (mt * 16 + (lane & 15)) * 8);
#pragma unroll
        for (int i = 0; i < 4; ++i) { const f32x4 v = sp[i]; a += v[0] + v[2]; q += v[1] + v[3]; }
        const float mean = a * (1.0f / 512.0f);
        const float var = fmaxf(q * (1.0f / 512.0f) - mean * mean, 0.f);
        const float rstd = __builtin_amdgcn_rsqf(var + 1e-5f);
#pragma unroll
        for (int i = 0; i < 16; ++i) f[mt][i] = (f[mt][i] - mean) * rstd;
        fr[mt][0] = pack8(f[mt]); fr[mt][1] = pack8(f[mt] + 8);
    }
}

template <int MODE>
DEVI void role_mlp(const Params& p, rsrc_t rs, char* lds, int tid, int par, int i) {
    const int lane = tid & 63, w = tid >> 6, g = lane >> 4, l16 = lane & 15;
    const int pr = w >> 1, mt = w & 1;
    u32x4 wa[2][16];
    {
        const __bf16* wsrc = MODE == 1 ? p.w1 : (MODE == 2 ? p.w2 : p.w3);
        constexpr int LD = MODE == 3 ? 1792 : 512;
        stage_rows<512, 128, false>(wsrc + 128 * i, LD, lds, tid);
        __syncthreads();
#pragma unroll
        for (int nt = 0; nt < 2; ++nt)
#pragma unroll
            for (int ks = 0; ks < 16; ++ks) wa[nt][ks] = tr_frag<128>(lds, ks, 32 * pr + 16 * nt, lane);
        __syncthreads();
    }
    char* zb = lds;
    f32x2* st0 = (f32x2*)(lds + 32768);
    f32x2* st1 = (f32x2*)(lds + 32768 + 2048);
    bool dead = false;
    for (int s = par; s < NSEG; s += 2) {
        u32x4 fr[2][2];
        u32x4 yv = {0u, 0u, 0u, 0u};
        if (MODE == 1) {
            wait_flags(rs, OFF_FLAG + (FL_H + par * 32 + (lane & 31)) * 4, true, (unsigned)(s + 1), dead, 4);
#pragma unroll
            for (int m = 0; m < 2; ++m)
#pragma unroll
                for (int k = 0; k < 2; ++k) fr[m][k] = ld128(rs, OFF_HF + ((2 * s + m) * 16 + 2 * w + k) * 1024 + lane * 16);
            coop_ln(fr, st0, lane, w);
            if ((w >> 1) == i) {
#pragma unroll
                for (int m = 0; m < 2; ++m)
#pragma unroll
                    for (int k = 0; k < 2; ++k) st128(rs, OFF_YF + ((2 * s + m) * 16 + 2 * w + k) * 1024 + lane * 16, fr[m][k]);
            }
            coop_ln(fr, st1, lane, w);
        } else if (MODE == 2) {
            const unsigned need = lane < 32 ? (unsigned)(s + 1) : (unsigned)(s + 1 < NSEG ? s + 2 : s);
            const int fo = FL_M1 + (lane < 32 ? par * 32 + lane : (par ^ 1) * 32 + (lane - 32));
            {
                if (!dead) {
                    for (unsigned sp = 0;; ++sp) {
                        unsigned f = ld32(rs, OFF_FLAG + fo * 4);
                        MEMBAR();
                        if (__all(f >= need)) break;
                        if (sp > SPIN_CAP) { dead = true; st32(rs, OFF_FLAG + FL_ERR * 4, 6u); break; }
                        __builtin_amdgcn_s_sleep(8);
                    }
                }
            }
            const int q = w >> 1;
            const int dl = q == 0 ? -1 : (q == 1 ? 1 : (q == 2 ? -2 : 2));
#pragma unroll
            for (int m = 0; m < 2; ++m) {
                const int T = 32 * s + 16 * m + l16 + dl;
                const bool ok = T >= 0 && T < 2048;
                const int Tc = ok ? T : 0;
#pragma unroll
                for (int k = 0; k < 2; ++k) {
                    u32x4 v = ld128(rs, OFF_H1F + ((Tc >> 4) * 16 + 2 * w + k) * 1024 + (16 * g + (Tc & 15)) * 16);
                    fr[m][k] = ok ? v : u32x4{0u, 0u, 0u, 0u};
                }
            }
            yv = ld128(rs, OFF_YF + ((2 * s + mt) * 16 + 4 * i + pr) * 1024 + lane * 16);
        } else {
            wait_flags(rs, OFF_FLAG + (FL_M2 + par * 32 + (lane & 31)) * 4, true, (unsigned)(s + 1), dead, 4);
#pragma unroll
            for (int m = 0; m < 2; ++m)
#pragma unroll
                for (int k = 0; k < 2; ++k) fr[m][k] = ld128(rs, OFF_X1F + ((2 * s + m) * 16 + 2 * w + k) * 1024 + lane * 16);
            coop_ln(fr, st0, lane, w);
        }
        if (MODE == 2) __syncthreads();
#pragma unroll
        for (int m = 0; m < 2; ++m)
#pragma unroll
            for (int k = 0; k < 2; ++k) *(u32x4*)(zb + (m * 16 + 2 * w + k) * 1024 + lane * 16) = fr[m][k];
        __syncthreads();
        f32x4 a0 = {0.f, 0.f, 0.f, 0.f}, a1 = a0;
#pragma unroll
        for (int ks = 0; ks < 16; ++ks) {
            const u32x4 b = *(const u32x4*)(zb + (mt * 16 + ks) * 1024 + lane * 16);
            a0 = mfma32(wa[0][ks], b, a0);
            a1 = mfma32(wa[1][ks], b, a1);
        }
        if (MODE == 2) {
            const u32x4 o = {pk2(bflo(yv.x) + rbf(a0[0]), bfhi(yv.x) + rbf(a0[1])), pk2(bflo(yv.y) + rbf(a0[2]), bfhi(yv.y) + rbf(a0[3])),
                             pk2(bflo(yv.z) + rbf(a1[0]), bfhi(yv.z) + rbf(a1[1])), pk2(bflo(yv.w) + rbf(a1[2]), bfhi(yv.w) + rbf(a1[3]))};
            st128(rs, OFF_X1F + ((2 * s + mt) * 16 + 4 * i + pr) * 1024 + lane * 16, o);
            WAIT_VM0();
            if (lane == 0) st32(rs, OFF_FLAG + (FL_M2 + par * 32 + i * 8 + w) * 4, (unsigned)(s + 1));
        } else {
            const u32x4 o = {pk2(gelu_t(rbf(a0[0])), gelu_t(rbf(a0[1]))), pk2(gelu_t(rbf(a0[2])), gelu_t(rbf(a0[3]))),
                             pk2(gelu_t(rbf(a1[0])), gelu_t(rbf(a1[1]))), pk2(gelu_t(rbf(a1[2])), gelu_t(rbf(a1[3])))};
            if (MODE == 1) st128(rs, OFF_H1F + ((2 * s + mt) * 16 + 4 * i + pr) * 1024 + lane * 16, o);
            else st128(rs, OFF_AF + ((2 * s + mt) * 56 + 4 * i + pr) * 1024 + lane * 16, o);
            WAIT_VM0();
            if (lane == 0) st32(rs, OFF_FLAG + (MODE == 1 ? FL_M1 + par * 32 + i * 8 + w : FL_M3 + par * 128 + i * 8 + w) * 4, (unsigned)(s + 1));
        }
    }
}

DEVI void role_m4(const Params& p, rsrc_t rs, char* lds, int tid, int par, int i) {
    const int lane = tid & 63, w = tid >> 6, g = lane >> 4, l16 = lane & 15;
    const int nt = w & 3, kh = w >> 2;
    u32x4 wa[28];
#pragma unroll
    for (int r = 0; r < 2; ++r) {
        stage_rows<896, 64, false>(p.w4 + (size_t)(896 * r) * 512 + 64 * i, 512, lds, tid);
        __syncthreads();
        if (kh == r) {
#pragma unroll
            for (int ks = 0; ks < 28; ++ks) wa[ks] = tr_frag<64>(lds, ks, 16 * nt, lane);
        }
        __syncthreads();
    }
    char* ab = lds;
    f32x4* red = (f32x4*)(lds + 114688);
    bool dead = false;
    for (int s = par; s < NSEG; s += 2) {
        {
            const int f0 = OFF_FLAG + (FL_M3 + par * 128 + lane) * 4;
            if (!dead) {
                for (unsigned sp = 0;; ++sp) {
                    unsigned fa = ld32(rs, f0), fb = ld32(rs, f0 + 256);
                    MEMBAR();
                    if (__all(fa >= (unsigned)(s + 1) && (lane >= 48 || fb >= (unsigned)(s + 1)))) break;
                    if (sp > SPIN_CAP) { dead = true; st32(rs, OFF_FLAG + FL_ERR * 4, 7u); break; }
                    __builtin_amdgcn_s_sleep(8);
                }
            }
        }
        u32x4 ar[14];
#pragma unroll
        for (int k = 0; k < 14; ++k) ar[k] = ld128(rs, OFF_AF + s * 114688 + k * 8192 + tid * 16);
        u32x2 x1v[2], hv[2];
        if (kh == 0) {
#pragma unroll
            for (int m = 0; m < 2; ++m) {
                const int o = ((2 * s + m) * 16 + 2 * i + (nt >> 1)) * 1024 + lane * 16 + (nt & 1) * 8;
                x1v[m] = ld64(rs, OFF_X1F + o); hv[m] = ld64(rs, OFF_HF + o);
            }
        }
        __syncthreads();
#pragma unroll
        for (int k = 0; k < 14; ++k) *(u32x4*)(ab + k * 8192 + tid * 16) = ar[k];
        __syncthreads();
        f32x4 acc[2][2];
#pragma unroll
        for (int m = 0; m < 2; ++m) { acc[m][0] = f32x4{0.f, 0.f, 0.f, 0.f}; acc[m][1] = acc[m][0]; }
#pragma unroll
        for (int ks = 0; ks < 28; ++ks) {
#pragma unroll
            for (int m = 0; m < 2; ++m) {
                const u32x4 b = *(const u32x4*)(ab + (m * 56 + 28 * kh + ks) * 1024 + lane * 16);
                acc[m][ks & 1] = mfma32(wa[ks], b, acc[m][ks & 1]);
            }
        }
        acc[0][0] += acc[0][1]; acc[1][0] += acc[1][1];
        if (kh == 1) { red[(nt * 2 + 0) * 64 + lane] = acc[0][0]; red[(nt * 2 + 1) * 64 + lane] = acc[1][0]; }
        __syncthreads();
        if (kh == 0) {
#pragma unroll
            for (int m = 0; m < 2; ++m) {
                const f32x4 d = acc[m][0] + red[(nt * 2 + m) * 64 + lane];
                const float t0 = rbf(bflo(x1v[m].x) + rbf(d[0])), t1 = rbf(bfhi(x1v[m].x) + rbf(d[1]));
                const float t2 = rbf(bflo(x1v[m].y) + rbf(d[2])), t3 = rbf(bfhi(x1v[m].y) + rbf(d[3]));
                const u32x2 o = {pk2(bflo(hv[m].x) + t0, bfhi(hv[m].x) + t1), pk2(bflo(hv[m].y) + t2, bfhi(hv[m].y) + t3)};
                *(u32x2*)(p.out + (size_t)(32 * s + 16 * m + l16) * 512 + 64 * i + 16 * nt + 4 * g) = o;
            }
        }
    }
}

__global__ void __launch_bounds__(512, 2) main_kernel(Params p) {
    extern __shared__ __attribute__((aligned(16))) char lds[];
    const int tid = threadIdx.x, b = blockIdx.x;
    const rsrc_t rs = __builtin_amdgcn_make_buffer_rsrc(p.ws, 0, WS_SPAN, 0x00020000);
    const int xcd = b & 7, slot = b >> 3;
    if (xcd == 2) role_score(p, rs, lds, tid, slot);
    else if (xcd < 2) role_col(p, rs, lds, tid, slot * 2 + xcd);
    else {
        const int wid = slot * 5 + (xcd - 3);
        if (wid == 155) role_probe(rs, lds, tid);
        else if (wid < 32) role_qk(p, rs, lds, tid, wid);
        else if (wid < 64) role_vu(p, rs, lds, tid, wid - 32);
        else if (wid < 96) role_tb2(p, rs, lds, tid, (wid - 64) & 1, (wid - 64) >> 1);
        else if (wid < 104) role_mlp<1>(p, rs, lds, tid, (wid - 96) & 1, (wid - 96) >> 1);
        else if (wid < 112) role_mlp<2>(p, rs, lds, tid, (wid - 104) & 1, (wid - 104) >> 1);
        else if (wid < 140) role_mlp<3>(p, rs, lds, tid, (wid - 112) & 1, (wid - 112) >> 1);
        else if (wid < 155) role_m4(p, rs, lds, tid, (wid - 140) & 1, (wid - 140) >> 1);
        else if (wid == 156) role_m4(p, rs, lds, tid, 1, 7);
    }
}

extern "C" void kernel_launch(void* const* d_in, const int* in_sizes, int n_in, void* d_out, int out_size, void* d_ws, size_t ws_size,
                              hipStream_t stream) {
    (void)in_sizes; (void)n_in; (void)out_size; (void)ws_size;
    Params p;
    p.x = (const __bf16*)d_in[0]; p.mem0 = (const __bf16*)d_in[1];
    p.wq = (const __bf16*)d_in[2]; p.wk = (const __bf16*)d_in[3]; p.wv = (const __bf16*)d_in[4]; p.wo = (const __bf16*)d_in[5];
    p.w1 = (const __bf16*)d_in[8]; p.w2 = (const __bf16*)d_in[9]; p.w3 = (const __bf16*)d_in[10]; p.w4 = (const __bf16*)d_in[11];
    p.out = (__bf16*)d_out;
    p.ws = (char*)(((uintptr_t)d_ws + 262143u) & ~(uintptr_t)262143u);
    hipFuncSetAttribute((const void*)prep_kernel, hipFuncAttributeMaxDynamicSharedMemorySize, LDS_BYTES);
    hipFuncSetAttribute((const void*)main_kernel, hipFuncAttributeMaxDynamicSharedMemorySize, LDS_BYTES);
    hipLaunchKernelGGL(prep_kernel, dim3(256), dim3(512), LDS_BYTES, stream, p);
    hipLaunchKernelGGL(main_kernel, dim3(256), dim3(512), LDS_BYTES, stream, p);
}
```

```cpp
#include <hip/hip_runtime.h>
#include <hip/hip_bf16.h>
#include <stdint.h>

typedef __bf16 bf16x2 __attribute__((ext_vector_type(2)));
typedef __bf16 bf16x4 __attribute__((ext_vector_type(4)));
typedef __bf16 bf16x8 __attribute__((ext_vector_type(8)));
typedef __bf16 bf16x16 __attribute__((ext_vector_type(16)));
typedef short s16x4 __attribute__((ext_vector_type(4)));
typedef float f32x2 __attribute__((ext_vector_type(2)));
typedef float f32x4 __attribute__((ext_vector_type(4)));
typedef unsigned u32x2 __attribute__((ext_vector_type(2)));
typedef unsigned u32x4 __attribute__((ext_vector_type(4)));
typedef unsigned u32x8 __attribute__((ext_vector_type(8)));
typedef __attribute__((address_space(3))) s16x4* lds_s16x4_ptr;
typedef __amdgpu_buffer_rsrc_t rsrc_t;

#define DEVI __device__ __forceinline__
#define MEMBAR() asm volatile("" ::: "memory")
#define WAIT_VM0() asm volatile("s_waitcnt vmcnt(0)" ::: "memory")

constexpr int NSEG = 64;
constexpr int SC1 = 16;
constexpr unsigned SPIN_CAP = 400000u;
constexpr unsigned POISON = 0xFFFFFFFFu;

constexpr int MB = 1 << 20;
constexpr int OFF_HO = 0;
constexpr int OFF_FLAG = 256 * 1024;
constexpr int OFF_XNF = 1 * MB;
constexpr int OFF_HF = 3 * MB;
constexpr int OFF_YF = 5 * MB;
constexpr int OFF_H1F = 7 * MB;
constexpr int OFF_X1F = 9 * MB;
constexpr int OFF_AF = 11 * MB;
constexpr int OFF_PX = 18 * MB;
constexpr int OFF_PM = 22 * MB;
constexpr int OFF_BIMG = 24 * MB;
constexpr int OFF_QX = 40 * MB;
constexpr int OFF_KX = 56 * MB;
constexpr int OFF_UM = 72 * MB;
constexpr int OFF_UX = 88 * MB;
constexpr int OFF_DBG = 152 * MB;
constexpr int WS_SPAN = 160 * MB;
constexpr int FL_SEL = 0;
constexpr int FL_ERR = 32;
constexpr int FL_QK = 64;
constexpr int FL_VU = 320;
constexpr int FL_P = 576;
constexpr int FL_U = 640;
constexpr int FL_INIT = 704;
constexpr int FL_WORDS = 1152;

constexpr int LDS_BYTES = 151552;

struct Params {
    const __bf16* x; const __bf16* mem0; const __bf16* wq; const __bf16* wk; const __bf16* wv; const __bf16* wo;
    const __bf16* w1; const __bf16* w2; const __bf16* w3; const __bf16* w4;
    __bf16* out; char* ws;
};

DEVI unsigned pk2(float a, float b) {
    f32x2 v = {a, b};
    bf16x2 r = __builtin_convertvector(v, bf16x2);
    return __builtin_bit_cast(unsigned, r);
}
DEVI float bflo(unsigned u) { return __builtin_bit_cast(float, u << 16); }
DEVI float bfhi(unsigned u) { return __builtin_bit_cast(float, u & 0xFFFF0000u); }
DEVI float rbf(float a) { return bflo(pk2(a, 0.f)); }
DEVI f32x4 mfma32(u32x4 a, u32x4 b, f32x4 c) {
    return __builtin_amdgcn_mfma_f32_16x16x32_bf16(__builtin_bit_cast(bf16x8, a), __builtin_bit_cast(bf16x8, b), c, 0, 0, 0);
}
DEVI f32x4 mfma16(u32x2 a, u32x2 b, f32x4 c) {
    return __builtin_amdgcn_mfma_f32_16x16x16bf16_1k(__builtin_bit_cast(s16x4, a), __builtin_bit_cast(s16x4, b), c, 0, 0, 0);
}
DEVI f32x4 smfmac(u32x4 a, u32x4 b0, u32x4 b1, f32x4 c, int idx) {
    u32x8 b = __builtin_shufflevector(b0, b1, 0, 1, 2, 3, 4, 5, 6, 7);
    return __builtin_amdgcn_smfmac_f32_16x16x64_bf16(__builtin_bit_cast(bf16x8, a), __builtin_bit_cast(bf16x16, b), c, idx, 0, 0);
}
DEVI u32x4 ld128(rsrc_t rs, int off) { return __builtin_amdgcn_raw_buffer_load_b128(rs, off, 0, SC1); }
DEVI u32x2 ld64(rsrc_t rs, int off) { return __builtin_amdgcn_raw_buffer_load_b64(rs, off, 0, SC1); }
DEVI unsigned ld32(rsrc_t rs, int off) { return __builtin_amdgcn_raw_buffer_load_b32(rs, off, 0, SC1); }
DEVI void st128(rsrc_t rs, int off, u32x4 v) { __builtin_amdgcn_raw_buffer_store_b128(v, rs, off, 0, SC1); }
DEVI void st64(rsrc_t rs, int off, u32x2 v) { __builtin_amdgcn_raw_buffer_store_b64(v, rs, off, 0, SC1); }
DEVI void st32(rsrc_t rs, int off, unsigned v) { __builtin_amdgcn_raw_buffer_store_b32(v, rs, off, 0, SC1); }
DEVI int amap(int x) { return (x & 8191) | ((x >> 13) << 14); }
DEVI unsigned sanit(unsigned u) { return u < 0xFFFFFFFEu ? u : 0xFFFFFFFEu; }

template <int CTRL> DEVI float dppf(float v) {
    return __builtin_bit_cast(float, __builtin_amdgcn_update_dpp(0, __builtin_bit_cast(int, v), CTRL, 0xF, 0xF, true));
}
template <int CTRL> DEVI unsigned dppu(unsigned v) {
    return (unsigned)__builtin_amdgcn_update_dpp(0, (int)v, CTRL, 0xF, 0xF, true);
}
constexpr int DPP_ROR1 = 0x121, DPP_ROR2 = 0x122, DPP_ROR4 = 0x124, DPP_ROR8 = 0x128;
constexpr int DPP_SHL2 = 0x102, DPP_SHL4 = 0x104, DPP_SHL6 = 0x106;
constexpr int DPP_XOR1 = 0xB1;

DEVI void swap32(unsigned& a, unsigned& b) { auto r = __builtin_amdgcn_permlane32_swap(a, b, false, false); a = r[0]; b = r[1]; }
DEVI void swap16(unsigned& a, unsigned& b) { auto r = __builtin_amdgcn_permlane16_swap(a, b, false, false); a = r[0]; b = r[1]; }
DEVI float fold32(float x, float y) {
    unsigned a = __builtin_bit_cast(unsigned, x), b = __builtin_bit_cast(unsigned, y);
    swap32(a, b);
    return __builtin_bit_cast(float, a) + __builtin_bit_cast(float, b);
}
DEVI float xg_sum(float v) {
    unsigned a = __builtin_bit_cast(unsigned, v), b = a; swap16(a, b);
    v = __builtin_bit_cast(float, a) + __builtin_bit_cast(float, b);
    a = __builtin_bit_cast(unsigned, v); b = a; swap32(a, b);
    return __builtin_bit_cast(float, a) + __builtin_bit_cast(float, b);
}
DEVI float xg_max(float v) {
    unsigned a = __builtin_bit_cast(unsigned, v), b = a; swap16(a, b);
    v = fmaxf(__builtin_bit_cast(float, a), __builtin_bit_cast(float, b));
    a = __builtin_bit_cast(unsigned, v); b = a; swap32(a, b);
    return fmaxf(__builtin_bit_cast(float, a), __builtin_bit_cast(float, b));
}
DEVI float wave_sum(float v) {
    v += dppf<DPP_ROR1>(v); v += dppf<DPP_ROR2>(v); v += dppf<DPP_ROR4>(v); v += dppf<DPP_ROR8>(v);
    return xg_sum(v);
}
DEVI float wave_max(float v) {
    v = fmaxf(v, dppf<DPP_ROR1>(v)); v = fmaxf(v, dppf<DPP_ROR2>(v)); v = fmaxf(v, dppf<DPP_ROR4>(v)); v = fmaxf(v, dppf<DPP_ROR8>(v));
    return xg_max(v);
}
DEVI float wave_max_u(float v) {
    float t;
    asm volatile("s_nop 1\n\t"
                 "v_max_f32_dpp %0, %1, %1 row_ror:8 row_mask:0xf bank_mask:0xf\n\ts_nop 1\n\t"
                 "v_max_f32_dpp %0, %0, %0 row_ror:4 row_mask:0xf bank_mask:0xf\n\ts_nop 1\n\t"
                 "v_max_f32_dpp %0, %0, %0 row_ror:2 row_mask:0xf bank_mask:0xf\n\ts_nop 1\n\t"
                 "v_max_f32_dpp %0, %0, %0 row_ror:1 row_mask:0xf bank_mask:0xf\n\ts_nop 1\n\t"
                 "v_max_f32_dpp %0, %0, %0 row_bcast:15 row_mask:0xa bank_mask:0xf\n\ts_nop 1\n\t"
                 "v_max_f32_dpp %0, %0, %0 row_bcast:31 row_mask:0xc bank_mask:0xf\n\ts_nop 1"
                 : "=&v"(t) : "v"(v));
    return __builtin_bit_cast(float, __builtin_amdgcn_readlane(__builtin_bit_cast(int, t), 63));
}
DEVI float wave_sum_u(float v) {
    float t;
    asm volatile("s_nop 1\n\t"
                 "v_add_f32_dpp %0, %1, %1 row_ror:8 row_mask:0xf bank_mask:0xf\n\ts_nop 1\n\t"
                 "v_add_f32_dpp %0, %0, %0 row_ror:4 row_mask:0xf bank_mask:0xf\n\ts_nop 1\n\t"
                 "v_add_f32_dpp %0, %0, %0 row_ror:2 row_mask:0xf bank_mask:0xf\n\ts_nop 1\n\t"
                 "v_add_f32_dpp %0, %0, %0 row_ror:1 row_mask:0xf bank_mask:0xf\n\ts_nop 1\n\t"
                 "v_add_f32_dpp %0, %0, %0 row_bcast:15 row_mask:0xa bank_mask:0xf\n\ts_nop 1\n\t"
                 "v_add_f32_dpp %0, %0, %0 row_bcast:31 row_mask:0xc bank_mask:0xf\n\ts_nop 1"
                 : "=&v"(t) : "v"(v));
    return __builtin_bit_cast(float, __builtin_amdgcn_readlane(__builtin_bit_cast(int, t), 63));
}
DEVI unsigned now32() { return (unsigned)__builtin_amdgcn_s_memtime(); }
DEVI void nap_until(unsigned t) { while ((int)(now32() - t) < 0) __builtin_amdgcn_s_sleep(1); }
#define SCHED0() __builtin_amdgcn_sched_barrier(0)
DEVI unsigned umax(unsigned a, unsigned b) { return a > b ? a : b; }
DEVI float gelu_t(float x) {
    const float u = x * (0.7978845608028654f + 0.0356774081363001f * x * x);
    const float e = __builtin_amdgcn_exp2f(-2.885390081777927f * u);
    return x * __builtin_amdgcn_rcpf(1.0f + e);
}

DEVI void wait_flags(rsrc_t rs, int off, bool act, unsigned v, bool& dead, int slp) {
    if (dead) return;
    for (unsigned sp = 0;; ++sp) {
        unsigned f = ld32(rs, off);
        MEMBAR();
        if (__all(!act || f >= v)) break;
        if (sp > SPIN_CAP) { dead = true; st32(rs, OFF_FLAG + FL_ERR * 4, 1u); break; }
        for (int i = 0; i < slp; ++i) __builtin_amdgcn_s_sleep(2);
    }
}

template <int NROWS, int NCOLS, bool PERM>
DEVI void stage_rows(const __bf16* __restrict__ src, int ld, char* lds, int tid) {
    constexpr int CPR = NCOLS / 8;
    constexpr int TOT = NROWS * CPR;
    constexpr int PER = TOT / 512;
    constexpr int PITCH = NCOLS * 2 + 32;
    u32x4 v[PER];
#pragma unroll
    for (int i = 0; i < PER; ++i) {
        const int c = tid + 512 * i, row = c / CPR, cc = c % CPR;
        v[i] = *(const u32x4*)(src + (size_t)row * ld + cc * 8);
    }
#pragma unroll
    for (int i = 0; i < PER; ++i) {
        const int c = tid + 512 * i, row = c / CPR, cc = c % CPR;
        int lrow = row;
        if (PERM) {
            const int d = row & 31;
            const int g = 2 * (d >> 4) + ((d >> 2) & 1), r = 2 * ((d >> 3) & 1) + (d & 1), hf = (d >> 1) & 1;
            lrow = (row & ~31) + 16 * hf + 4 * g + r;
        }
        *(u32x4*)(lds + lrow * PITCH + cc * 16) = v[i];
    }
}
template <int NCOLS>
DEVI u32x4 tr_frag(const char* lds, int ks, int col0, int lane) {
    constexpr int PITCH = NCOLS * 2 + 32;
    const int g = lane >> 4, i = lane & 15;
    const char* p = lds + (32 * ks + 4 * g + (i >> 2)) * PITCH + (col0 + 4 * (i & 3)) * 2;
    s16x4 a = __builtin_amdgcn_ds_read_tr16_b64_v4i16((lds_s16x4_ptr)p);
    s16x4 b = __builtin_amdgcn_ds_read_tr16_b64_v4i16((lds_s16x4_ptr)(p + 16 * PITCH));
    u32x2 ua = __builtin_bit_cast(u32x2, a), ub = __builtin_bit_cast(u32x2, b);
    return u32x4{ua.x, ua.y, ub.x, ub.y};
}

__global__ void __launch_bounds__(512, 2) prep_kernel(Params p) {
    extern __shared__ __attribute__((aligned(16))) char lds[];
    const int tid = threadIdx.x, lane = tid & 63, w = tid >> 6, b = blockIdx.x;
    const int h = b & 31, t = b >> 5;
    char* ws = p.ws;
    if (b < 32) {
        const int phys = (b * 512 + tid) * 16;
        u32x4 v = {POISON, POISON, POISON, POISON};
        if (phys < 16384) {
            const int o = phys & 8191, j = o >> 7, r = (o >> 4) & 7;
            v = *(const u32x4*)(p.mem0 + r * 512 + 8 * j);
            v.x = sanit(v.x); v.y = sanit(v.y); v.z = sanit(v.z); v.w = sanit(v.w);
        }
        *(u32x4*)(ws + OFF_HO + phys) = v;
    } else if (b < 40) {
        const int i = (b - 32) * 512 + tid;
        if (i < FL_WORDS) *(unsigned*)(ws + OFF_FLAG + i * 4) = 0u;
    }
    stage_rows<128, 512, false>(p.wo + (size_t)h * 128 * 512, 512, lds, tid);
    u32x4 av[4][4];
    {
        const int g = lane >> 4;
#pragma unroll
        for (int dt = 0; dt < 4; ++dt)
#pragma unroll
            for (int ks = 0; ks < 4; ++ks) {
                const __bf16* src = p.wv + (size_t)(64 * t + 16 * dt + (lane & 15)) * 4096 + h * 128 + 32 * ks + 4 * g;
                u32x2 lo = *(const u32x2*)src, hi = *(const u32x2*)(src + 16);
                av[dt][ks] = u32x4{lo.x, lo.y, hi.x, hi.y};
            }
    }
    {
        const int T = 8 * b + w, ks = lane >> 2, g = lane & 3;
        const __bf16* src = p.x + (size_t)T * 512 + 32 * ks + 4 * g;
        u32x2 lo = *(const u32x2*)src, hi = *(const u32x2*)(src + 16);
        float f[8] = {bflo(lo.x), bfhi(lo.x), bflo(lo.y), bfhi(lo.y), bflo(hi.x), bfhi(hi.x), bflo(hi.y), bfhi(hi.y)};
        float s = 0.f;
#pragma unroll
        for (int i = 0; i < 8; ++i) s += f[i];
        const float mean = wave_sum(s) * (1.0f / 512.0f);
        float q = 0.f;
#pragma unroll
        for (int i = 0; i < 8; ++i) { f[i] -= mean; q += f[i] * f[i]; }
        const float rstd = __builtin_amdgcn_rsqf(wave_sum(q) * (1.0f / 512.0f) + 1e-5f);
        u32x4 o = {pk2(f[0] * rstd, f[1] * rstd), pk2(f[2] * rstd, f[3] * rstd), pk2(f[4] * rstd, f[5] * rstd), pk2(f[6] * rstd, f[7] * rstd)};
        *(u32x4*)(ws + OFF_XNF + ((T >> 4) * 16 + ks) * 1024 + (16 * g + (T & 15)) * 16) = o;
    }
    __syncthreads();
#pragma unroll
    for (int ci = 0; ci < 4; ++ci) {
        const int ct = 4 * w + ci;
        u32x4 bw[4];
#pragma unroll
        for (int ks = 0; ks < 4; ++ks) bw[ks] = tr_frag<512>(lds, ks, 16 * ct, lane);
        f32x4 acc[4];
#pragma unroll
        for (int dt = 0; dt < 4; ++dt) {
            acc[dt] = f32x4{0.f, 0.f, 0.f, 0.f};
#pragma unroll
            for (int ks = 0; ks < 4; ++ks) acc[dt] = mfma32(av[dt][ks], bw[ks], acc[dt]);
        }
#pragma unroll
        for (int half = 0; half < 2; ++half) {
            unsigned x0 = pk2(acc[2 * half][0], acc[2 * half][1]), x1 = pk2(acc[2 * half][2], acc[2 * half][3]);
            unsigned y0 = pk2(acc[2 * half + 1][0], acc[2 * half + 1][1]), y1 = pk2(acc[2 * half + 1][2], acc[2 * half + 1][3]);
            swap32(x0, y0); swap32(x1, y1);
            const int c16 = lane & 15, bb = (lane >> 4) & 1, tile = lane >> 5;
            const int gB = 2 * tile + bb, j = 2 * ct + (c16 >> 3), u = c16 & 7;
            const int n = 8 * (h & 1) + u;
            const size_t off = (((((size_t)j * 8 + (h >> 2)) * 2 + ((h >> 1) & 1)) * 8 + t) * 2 + half) * 1024 + (16 * gB + n) * 16;
            *(u32x4*)(ws + OFF_BIMG + off) = u32x4{x0, y0, x1, y1};
        }
    }
}

DEVI void role_probe(rsrc_t rs, char* lds, int tid) {
    const int lane = tid & 63, w = tid >> 6;
    unsigned* t = (unsigned*)lds;
    if (w < 2) {
        unsigned best = 0xFFFFFFFFu, acc = 0;
        for (int rep = 0; rep < 5; ++rep) {
            unsigned long long c0 = __builtin_amdgcn_s_memtime();
            unsigned v = ld32(rs, OFF_HO + 0x30000 + w * 8192 + rep * 256 + (acc & 0));
            asm volatile("s_waitcnt vmcnt(0)" ::"v"(v) : "memory");
            unsigned long long c1 = __builtin_amdgcn_s_memtime();
            v = ld32(rs, OFF_HO + 0x30000 + w * 8192 + rep * 256 + (v & 0));
            asm volatile("s_waitcnt vmcnt(0)" ::"v"(v) : "memory");
            unsigned long long c2 = __builtin_amdgcn_s_memtime();
            acc += v;
            const unsigned d = (unsigned)(c2 - c1);
            best = d < best ? d : best;
            (void)c0;
        }
        if (lane == 0) t[w] = best + (acc & 0);
    }
    __syncthreads();
    if (tid == 0) {
        const unsigned xcc = __builtin_amdgcn_s_getreg((20 << 0) | (0 << 6) | (3 << 11)) & 15u;
        unsigned near1 = t[1] < t[0] ? 1u : 0u;
        if (xcc >= 4) near1 ^= 1u;
        st32(rs, OFF_FLAG + FL_SEL * 4, 1u + near1);
    }
}

DEVI int get_base(rsrc_t rs, bool& dead) {
    unsigned f = 0;
    for (unsigned sp = 0;; ++sp) {
        f = ld32(rs, OFF_FLAG + FL_SEL * 4);
        MEMBAR();
        f = __builtin_amdgcn_readfirstlane(f);
        if (f != 0) break;
        if (sp > SPIN_CAP) { dead = true; st32(rs, OFF_FLAG + FL_ERR * 4, 2u); break; }
        __builtin_amdgcn_s_sleep(4);
    }
    return f == 2 ? 8192 : 0;
}
DEVI unsigned mx4(u32x4 v) { return umax(umax(v.x, v.y), umax(v.z, v.w)); }
DEVI bool okw(u32x4 v) { return mx4(v) != POISON; }
constexpr int HO_MEM = 0, HO_P = 32768, HO_PSTR = 24576;

#define DLY_S 2200
#define DLY_C1 1800
#define DLY_C2 600
#define THROTTLE(s) do { } while (0)
#define TS(ev) do { } while (0)
#define TSC(ev, mask) do { } while (0)
DEVI void nap(int n) { for (int i = 0; i < n; ++i) __builtin_amdgcn_s_sleep(1); }

DEVI void role_score(const Params& p, rsrc_t rs, char* lds, int tid, int h) {
    const int lane = tid & 63, w = tid >> 6, g = lane >> 4, l16 = lane & 15;
    u32x4 wq[16], wk[16];
    stage_rows<512, 128, true>(p.wq + h * 128, 4096, lds, tid);
    __syncthreads();
#pragma unroll
    for (int ks = 0; ks < 16; ++ks) wq[ks] = tr_frag<128>(lds, ks, 16 * w, lane);
    __syncthreads();
    stage_rows<512, 128, true>(p.wk + h * 128, 4096, lds, tid);
    __syncthreads();
#pragma unroll
    for (int ks = 0; ks < 16; ++ks) wk[ks] = tr_frag<128>(lds, ks, 16 * w, lane);
    __syncthreads();
    char* membuf = lds;
    char* kbuf = lds + 8192;
    char* qbuf = lds + 8192 + 16384;
    char* ybuf = lds + 8192 + 32768;
    float* sbuf = (float*)(lds + 8192 + 32768 + 8192);
    const int idx = l16 < 8 ? 0x4444 : 0xEEEE;
    u32x2 ident;
    {
        unsigned e[4];
#pragma unroll
        for (int j = 0; j < 4; ++j) e[j] = (4 * g + j == l16) ? 0x3F80u : 0u;
        ident = u32x2{e[0] | (e[1] << 16), e[2] | (e[3] << 16)};
    }
    bool dead = false;
    const int base = get_base(rs, dead);
    const int fqk = OFF_FLAG + (FL_QK + h * 8 + w) * 4;
    wait_flags(rs, fqk, true, 1u, dead, 2);
    unsigned cap = dead ? 0u : SPIN_CAP;
    u32x4 kx = ld128(rs, OFF_KX + (h * 8 + w) * 1024 + lane * 16);
    u32x4 qx = ld128(rs, OFF_QX + (h * 8 + w) * 1024 + lane * 16);
    unsigned fl = ld32(rs, fqk), avail = 1u;
    const float sc2 = 0.08838834764831845f * 1.4426950408889634f;
    const int pmo = OFF_HO + base + w * 1024 + lane * 16;
    const int lo = lane < 32 ? h * 512 + w * 64 + (lane >> 3) * 16 : 16384 + h * 128 + w * 16;
    const int sro = (lane < 40 ? lane : 0) + w * 44;
    const float lmask = lane < 40 ? 0.f : -INFINITY;
    unsigned tpoll = now32();
    for (int s = 0; s < NSEG; ++s) {
        const int par = s & 1;
        *(u32x4*)(kbuf + par * 8192 + w * 1024 + lane * 16) = kx;
        *(u32x4*)(qbuf + par * 8192 + w * 1024 + lane * 16) = qx;
        avail = umax(avail, __builtin_amdgcn_readfirstlane(fl));
        nap_until(tpoll);
        u32x4 v;
        {
            const int off = pmo + (s & 3) * 16384;
            for (unsigned sp = 0;; ++sp) {
                v = ld128(rs, off);
                MEMBAR();
                if (__all(okw(v))) break;
                if (sp >= cap) { if (cap) st32(rs, OFF_FLAG + FL_ERR * 4, 3u); cap = 0; break; }
            }
        }
        TSC(0, TSM_S);
        *(u32x4*)(membuf + w * 1024 + lane * 16) = v;
        __syncthreads();
        TSC(1, TSM_S);
        u32x4 a[8];
#pragma unroll
        for (int t = 0; t < 8; ++t) a[t] = *(const u32x4*)(membuf + t * 1024 + lane * 16);
        SCHED0();
        f32x4 aq = {0.f, 0.f, 0.f, 0.f}, ak = {0.f, 0.f, 0.f, 0.f};
#pragma unroll
        for (int t = 0; t < 8; ++t) {
            aq = smfmac(a[t], wq[2 * t], wq[2 * t + 1], aq, idx);
            ak = smfmac(a[t], wk[2 * t], wk[2 * t + 1], ak, idx);
        }
        f32x4 X;
#pragma unroll
        for (int i = 0; i < 4; ++i) {
            X[i] = fold32(aq[i], ak[i]);
        }
        const u32x2 xb = {pk2(X[0], X[1]), pk2(X[2], X[3])};
        const f32x4 yt = mfma16(xb, ident, f32x4{0.f, 0.f, 0.f, 0.f});
        const u32x2 y = {pk2(yt[0], yt[1]), pk2(yt[2], yt[3])};
        if (s < NSEG - 1) {
            const f32x4 z4 = {0.f, 0.f, 0.f, 0.f};
            const f32x4 s0 = mfma16(u32x2{kx.x, kx.y}, y, z4);
            const f32x4 s1 = mfma16(u32x2{kx.z, kx.w}, y, z4);
            const f32x4 sm = mfma16(y, y, z4);
            if (l16 < 8) {
                float* d = sbuf + (w * 8 + l16) * 44;
                *(f32x4*)(d + 8 * g) = s0;
                *(f32x4*)(d + 8 * g + 4) = s1;
                if (g >= 2) *(f32x4*)(d + 32 + 4 * (g - 2)) = sm;
            }
        }
        *(u32x2*)(ybuf + par * 4096 + w * 512 + lane * 8) = y;
        TSC(2, TSM_S);
        __syncthreads();
        TSC(3, TSM_S);
        if (s < NSEG - 1) {
            float pv[8];
#pragma unroll
            for (int i = 0; i < 8; ++i) pv[i] = sbuf[i * 8 * 44 + sro];
            float sv = ((pv[0] + pv[1]) + (pv[2] + pv[3])) + ((pv[4] + pv[5]) + (pv[6] + pv[7]));
            sv = __builtin_fmaf(sv, sc2, lmask);
            const float mx = wave_max_u(sv);
            const float e = __builtin_amdgcn_exp2f(sv - mx);
            const float sum = wave_sum_u(e);
            const float pr = e * __builtin_amdgcn_rcpf(sum);
            const unsigned d0 = sanit(pk2(pr, dppf<DPP_XOR1>(pr)));
            const u32x4 o = {d0, dppu<DPP_SHL2>(d0), dppu<DPP_SHL4>(d0), dppu<DPP_SHL6>(d0)};
            if ((lane & 7) == 0 && lane <= 32) {
                st128(rs, OFF_HO + base + amap(HO_P + (s & 3) * HO_PSTR + lo), o);
                SCHED0();
                tpoll = now32() + DLY_S;
                st128(rs, OFF_HO + base + amap(HO_P + ((s + 2) & 3) * HO_PSTR + lo), u32x4{POISON, POISON, POISON, POISON});
            }
            tpoll = __builtin_amdgcn_readfirstlane(tpoll);
            TSC(4, TSM_S);
            if (s > 0 && w < 2 && lane == 0) st32(rs, OFF_FLAG + (FL_P + h * 2 + w) * 4, (unsigned)s);
            if (avail < (unsigned)(s + 2)) { wait_flags(rs, fqk, true, (unsigned)(s + 2), dead, 2); avail = s + 2; if (dead) cap = 0; }
            kx = ld128(rs, OFF_KX + (((s + 1) * 32 + h) * 8 + w) * 1024 + lane * 16);
            qx = ld128(rs, OFF_QX + (((s + 1) * 32 + h) * 8 + w) * 1024 + lane * 16);
            fl = ld32(rs, fqk);
            SCHED0();
        } else {
            if (w < 2 && lane == 0) st32(rs, OFF_FLAG + (FL_P + h * 2 + w) * 4, (unsigned)s);
        }
        TSC(5, TSM_S);
        if (w < 2) {
            f32x4 dm = {0.f, 0.f, 0.f, 0.f}, d0 = dm, d1 = dm;
#pragma unroll
            for (int t = 0; t < 8; ++t) {
                const u32x4 kf = *(const u32x4*)(kbuf + par * 8192 + t * 1024 + lane * 16);
                const u32x2 qf = *(const u32x2*)(qbuf + par * 8192 + t * 1024 + lane * 16 + w * 8);
                const u32x2 yf = *(const u32x2*)(ybuf + par * 4096 + t * 512 + lane * 8);
                dm = mfma16(yf, qf, dm);
                d0 = mfma16(u32x2{kf.x, kf.y}, qf, d0);
                d1 = mfma16(u32x2{kf.z, kf.w}, qf, d1);
            }
            const int tok = 16 * w + l16;
            float mx = -INFINITY;
#pragma unroll
            for (int i = 0; i < 4; ++i) {
                dm[i] = g >= 2 ? dm[i] * sc2 : -INFINITY;
                d0[i] = (4 * g + i <= tok) ? d0[i] * sc2 : -INFINITY;
                d1[i] = (16 + 4 * g + i <= tok) ? d1[i] * sc2 : -INFINITY;
                mx = fmaxf(mx, fmaxf(dm[i], fmaxf(d0[i], d1[i])));
            }
            mx = xg_max(mx);
            float sum = 0.f;
#pragma unroll
            for (int i = 0; i < 4; ++i) {
                dm[i] = __builtin_amdgcn_exp2f(dm[i] - mx); d0[i] = __builtin_amdgcn_exp2f(d0[i] - mx); d1[i] = __builtin_amdgcn_exp2f(d1[i] - mx);
                sum += dm[i] + d0[i] + d1[i];
            }
            const float r = __builtin_amdgcn_rcpf(xg_sum(sum));
            st128(rs, OFF_PX + ((s * 32 + h) * 2 + w) * 1024 + lane * 16,
                  u32x4{pk2(d0[0] * r, d0[1] * r), pk2(d0[2] * r, d0[3] * r), pk2(d1[0] * r, d1[1] * r), pk2(d1[2] * r, d1[3] * r)});
            if (g >= 2) st64(rs, OFF_PM + (s * 32 + h) * 512 + tok * 16 + (g - 2) * 8, u32x2{pk2(dm[0] * r, dm[1] * r), pk2(dm[2] * r, dm[3] * r)});
            if (s == NSEG - 1) { WAIT_VM0(); if (lane == 0) st32(rs, OFF_FLAG + (FL_P + h * 2 + w) * 4, (unsigned)NSEG); }
        }
        TSC(6, TSM_S);
    }
}

DEVI void role_col(const Params& p, rsrc_t rs, char* lds, int tid, int j) {
    const int lane = tid & 63, w = tid >> 6, g = lane >> 4, l16 = lane & 15;
    u32x4 bi[2][16];
    {
        const char* src = p.ws + OFF_BIMG + (size_t)(j * 8 + w) * 32768 + lane * 16;
#pragma unroll
        for (int ii = 0; ii < 2; ++ii)
#pragma unroll
            for (int k = 0; k < 16; ++k) bi[ii][k] = *(const u32x4*)(src + (ii * 16 + k) * 1024);
    }
    char* membuf = lds;
    float* red = (float*)(lds + 8192);
    const int idx = l16 < 8 ? 0x4444 : 0xEEEE;
    bool dead = false;
    const int base = get_base(rs, dead);
    const int fvu = OFF_FLAG + (FL_VU + (j >> 3) * 32 + 4 * w + (lane & 3)) * 4;
    const int uoff = OFF_UX + ((4 * w + (l16 >> 3)) * 32 + (j >> 1)) * 1024 + (g * 16 + 8 * (j & 1) + (l16 & 7)) * 16;
    const int pmo = OFF_HO + base + w * 1024 + lane * 16;
    const int po0 = (4 * w + (l16 >> 3)) * 512 + (l16 & 7) * 64 + g * 16;
    const int pom = 16384 + (4 * w + 2 * (g >> 1) + (l16 >> 3)) * 128 + (l16 & 7) * 16 + (g & 1) * 8;
    const int umo = OFF_UM + ((4 * w + 2 * (g >> 1) + (l16 >> 3)) * 512 + 8 * j + (l16 & 7)) * 16 + (g & 1) * 8;
    float* rw = red + ((4 * (g & 1)) * 8 + (l16 & 7)) * 20 + w * 2 + (g >> 1);
    unsigned cap = dead ? 0u : SPIN_CAP;
    unsigned fl = 0;
    unsigned tpm = now32();
    for (int s = 0; s < NSEG; ++s) {
        nap_until(tpm);
        u32x4 v;
        {
            const int off = pmo + (s & 3) * 16384;
            for (unsigned sp = 0;; ++sp) {
                v = ld128(rs, off);
                MEMBAR();
                if (__all(okw(v))) break;
                if (sp >= cap) { if (cap) st32(rs, OFF_FLAG + FL_ERR * 4, 4u); cap = 0; break; }
            }
        }
        const unsigned tpp = now32() + DLY_C1;
        TSC(0, TSM_C);
        *(u32x4*)(membuf + w * 1024 + lane * 16) = v;
        u32x4 ux0 = {0u, 0u, 0u, 0u}, ux1 = ux0;
        if (s < NSEG - 1) {
            if (!__all(fl >= (unsigned)(s + 1))) { wait_flags(rs, fvu, true, (unsigned)(s + 1), dead, 2); if (dead) cap = 0; }
            ux0 = ld128(rs, uoff + s * MB); ux1 = ld128(rs, uoff + s * MB + 2 * 32768);
            fl = ld32(rs, fvu);
        }
        SCHED0();
        __syncthreads();
        TSC(1, TSM_C);
        u32x4 a[8];
#pragma unroll
        for (int t = 0; t < 8; ++t) a[t] = *(const u32x4*)(membuf + t * 1024 + lane * 16);
        SCHED0();
        f32x4 a0 = {0.f, 0.f, 0.f, 0.f}, a1 = a0;
#pragma unroll
        for (int t = 0; t < 8; ++t) {
            a0 = smfmac(a[t], bi[0][2 * t], bi[0][2 * t + 1], a0, idx);
            a1 = smfmac(a[t], bi[1][2 * t], bi[1][2 * t + 1], a1, idx);
        }
        f32x4 X;
#pragma unroll
        for (int i = 0; i < 4; ++i) {
            X[i] = fold32(a0[i], a1[i]);
        }
        const u32x2 xb = {pk2(X[0], X[1]), pk2(X[2], X[3])};
        st64(rs, umo + s * (256 * 1024), xb);
        if (s == NSEG - 1) {
            WAIT_VM0();
            __syncthreads();
            if (tid == 0) st32(rs, OFF_FLAG + (FL_U + j) * 4, (unsigned)NSEG);
            break;
        }
        TSC(2, TSM_C);
        nap_until(tpp);
        u32x4 p0, p1; u32x2 pm;
        {
            const int pb = HO_P + (s & 3) * HO_PSTR;
            const int o0 = OFF_HO + base + amap(pb + po0);
            const int o1 = OFF_HO + base + amap(pb + po0 + 1024);
            const int om = OFF_HO + base + amap(pb + pom);
            for (unsigned sp = 0;; ++sp) {
                p0 = ld128(rs, o0); p1 = ld128(rs, o1); pm = ld64(rs, om);
                MEMBAR();
                if (__all(umax(umax(mx4(p0), mx4(p1)), umax(pm.x, pm.y)) != POISON)) break;
                if (sp >= cap) { if (cap) st32(rs, OFF_FLAG + FL_ERR * 4, 5u); cap = 0; break; }
            }
        }
        TSC(3, TSM_C);
        f32x4 D = mfma32(p0, ux0, f32x4{0.f, 0.f, 0.f, 0.f});
        const f32x4 Dm = mfma16(pm, xb, f32x4{0.f, 0.f, 0.f, 0.f});
        D = mfma32(p1, ux1, D);
        D += Dm;
        if ((l16 < 8) == (g < 2)) {
#pragma unroll
            for (int i = 0; i < 4; ++i) rw[i * 160] = D[i];
        }
        __syncthreads();
        TSC(4, TSM_C);
        if (w == 0) {
            const f32x4* rp = (const f32x4*)(red + lane * 20);
            const f32x4 r0 = rp[0], r1 = rp[1], r2 = rp[2], r3 = rp[3];
            const f32x4 r4 = (r0 + r1) + (r2 + r3);
            const float sv = (r4[0] + r4[1]) + (r4[2] + r4[3]);
            const unsigned d0 = sanit(pk2(sv, dppf<DPP_XOR1>(sv)));
            const u32x4 o = {d0, dppu<DPP_SHL2>(d0), dppu<DPP_SHL4>(d0), dppu<DPP_SHL6>(d0)};
            if ((lane & 7) == 0) {
                const int r = lane >> 3;
                st128(rs, OFF_HO + base + ((s + 1) & 3) * 16384 + j * 128 + r * 16, o);
                SCHED0();
                st128(rs, OFF_HO + base + ((s + 3) & 3) * 16384 + j * 128 + r * 16, u32x4{POISON, POISON, POISON, POISON});
            }
            if (lane == 0) st32(rs, OFF_FLAG + (FL_U + j) * 4, (unsigned)(s + 1));
        }
        tpm = now32() + DLY_C2;
        TSC(5, TSM_C);
    }
}

DEVI void role_qk(const Params& p, rsrc_t rs, char* lds, int tid, int h) {
    const int lane = tid & 63, w = tid >> 6;
    u32x4 wq[16], wk[16];
    stage_rows<512, 128, false>(p.wq + h * 128, 4096, lds, tid);
    __syncthreads();
#pragma unroll
    for (int ks = 0; ks < 16; ++ks) wq[ks] = tr_frag<128>(lds, ks, 16 * w, lane);
    __syncthreads();
    stage_rows<512, 128, false>(p.wk + h * 128, 4096, lds, tid);
    __syncthreads();
#pragma unroll
    for (int ks = 0; ks < 16; ++ks) wk[ks] = tr_frag<128>(lds, ks, 16 * w, lane);
    __syncthreads();
    const char* xn = p.ws + OFF_XNF + tid * 16;
    u32x4 xr[4];
#pragma unroll
    for (int i = 0; i < 4; ++i) xr[i] = *(const u32x4*)(xn + i * 8192);
    const int fo = OFF_FLAG + (FL_QK + h * 8 + w) * 4;
    for (int s = 0; s < NSEG; ++s) {
        TS(0);
        char* xb = lds + (s & 1) * 32768;
#pragma unroll
        for (int i = 0; i < 4; ++i) *(u32x4*)(xb + i * 8192 + tid * 16) = xr[i];
        MEMBAR();
        if (s >= 3 && s < NSEG - 1 && lane == 0) st32(rs, fo, (unsigned)(s - 1));
        __syncthreads();
        {
            const int sn = s + 1 < NSEG ? s + 1 : s;
#pragma unroll
            for (int i = 0; i < 4; ++i) xr[i] = *(const u32x4*)(xn + sn * 32768 + i * 8192);
        }
        THROTTLE(s);
        SCHED0();
        f32x4 q0 = {0.f, 0.f, 0.f, 0.f}, q1 = q0, k0 = q0, k1 = q0;
        u32x4 fb[3][4];
        const char* xl = xb + lane * 16;
#define QK_LD(c) do { fb[(c) % 3][0] = *(const u32x4*)(xl + (2 * (c)) * 1024); fb[(c) % 3][1] = *(const u32x4*)(xl + 16384 + (2 * (c)) * 1024); \
                      fb[(c) % 3][2] = *(const u32x4*)(xl + (2 * (c) + 1) * 1024); fb[(c) % 3][3] = *(const u32x4*)(xl + 16384 + (2 * (c) + 1) * 1024); } while (0)
        QK_LD(0); QK_LD(1);
#pragma unroll
        for (int c = 0; c < 8; ++c) {
            if (c + 2 < 8) QK_LD(c + 2);
            SCHED0();
            q0 = mfma32(wq[2 * c], fb[c % 3][0], q0); q1 = mfma32(wq[2 * c], fb[c % 3][1], q1);
            k0 = mfma32(wk[2 * c], fb[c % 3][0], k0); k1 = mfma32(wk[2 * c], fb[c % 3][1], k1);
            q0 = mfma32(wq[2 * c + 1], fb[c % 3][2], q0); q1 = mfma32(wq[2 * c + 1], fb[c % 3][3], q1);
            k0 = mfma32(wk[2 * c + 1], fb[c % 3][2], k0); k1 = mfma32(wk[2 * c + 1], fb[c % 3][3], k1);
            SCHED0();
        }
        const int o = ((s * 32 + h) * 8 + w) * 1024 + lane * 16;
        st128(rs, OFF_QX + o, u32x4{pk2(q0[0], q0[1]), pk2(q0[2], q0[3]), pk2(q1[0], q1[1]), pk2(q1[2], q1[3])});
        st128(rs, OFF_KX + o, u32x4{pk2(k0[0], k0[1]), pk2(k0[2], k0[3]), pk2(k1[0], k1[1]), pk2(k1[2], k1[3])});
        TS(1);
        if (s < 2 || s >= NSEG - 2) { WAIT_VM0(); if (lane == 0) st32(rs, fo, (unsigned)(s + 1)); }
    }
}

DEVI void role_vu(const Params& p, rsrc_t rs, char* lds, int tid, int h) {
    const int lane = tid & 63, w = tid >> 6;
    u32x4 wv[16], wo[16];
    stage_rows<512, 128, false>(p.wv + h * 128, 4096, lds, tid);
    __syncthreads();
#pragma unroll
    for (int ks = 0; ks < 16; ++ks) wv[ks] = tr_frag<128>(lds, ks, 16 * w, lane);
    __syncthreads();
    stage_rows<128, 512, false>(p.wo + (size_t)h * 128 * 512, 512, lds, tid);
    __syncthreads();
#pragma unroll
    for (int ci = 0; ci < 4; ++ci)
#pragma unroll
        for (int ks = 0; ks < 4; ++ks) wo[ci * 4 + ks] = tr_frag<512>(lds, ks, 16 * (4 * w + ci), lane);
    __syncthreads();
    const char* xn = p.ws + OFF_XNF + tid * 16;
    u32x4 xr[4];
#pragma unroll
    for (int i = 0; i < 4; ++i) xr[i] = *(const u32x4*)(xn + i * 8192);
    char* vbuf = lds + 65536;
    const int fo = OFF_FLAG + (FL_VU + w * 32 + h) * 4;
    for (int s = 0; s < NSEG; ++s) {
        TS(0);
        char* xb = lds + (s & 1) * 32768;
#pragma unroll
        for (int i = 0; i < 4; ++i) *(u32x4*)(xb + i * 8192 + tid * 16) = xr[i];
        MEMBAR();
        if (s >= 3 && s < NSEG - 1 && lane == 0) st32(rs, fo, (unsigned)(s - 1));
        __syncthreads();
        {
            const int sn = s + 1 < NSEG ? s + 1 : s;
#pragma unroll
            for (int i = 0; i < 4; ++i) xr[i] = *(const u32x4*)(xn + sn * 32768 + i * 8192);
        }
        THROTTLE(s);
        SCHED0();
        f32x4 v0 = {0.f, 0.f, 0.f, 0.f}, v1 = v0, v0b = v0, v1b = v0;
        u32x4 fb[3][4];
        const char* xl = xb + lane * 16;
        QK_LD(0); QK_LD(1);
#pragma unroll
        for (int c = 0; c < 8; ++c) {
            if (c + 2 < 8) QK_LD(c + 2);
            SCHED0();
            v0 = mfma32(wv[2 * c], fb[c % 3][0], v0); v1 = mfma32(wv[2 * c], fb[c % 3][1], v1);
            v0b = mfma32(wv[2 * c + 1], fb[c % 3][2], v0b); v1b = mfma32(wv[2 * c + 1], fb[c % 3][3], v1b);
            SCHED0();
        }
        v0 += v0b; v1 += v1b;
        *(u32x2*)(vbuf + (w >> 1) * 1024 + lane * 16 + (w & 1) * 8) = u32x2{pk2(v0[0], v0[1]), pk2(v0[2], v0[3])};
        *(u32x2*)(vbuf + 4096 + (w >> 1) * 1024 + lane * 16 + (w & 1) * 8) = u32x2{pk2(v1[0], v1[1]), pk2(v1[2], v1[3])};
        __syncthreads();
        u32x4 va[2][4];
#pragma unroll
        for (int ks = 0; ks < 4; ++ks)
#pragma unroll
            for (int mt = 0; mt < 2; ++mt) va[mt][ks] = *(const u32x4*)(vbuf + mt * 4096 + ks * 1024 + lane * 16);
        SCHED0();
        f32x4 u[4][2];
#pragma unroll
        for (int ci = 0; ci < 4; ++ci)
#pragma unroll
            for (int mt = 0; mt < 2; ++mt) {
                u[ci][mt] = f32x4{0.f, 0.f, 0.f, 0.f};
            }
#pragma unroll
        for (int ks = 0; ks < 4; ++ks)
#pragma unroll
            for (int ci = 0; ci < 4; ++ci)
#pragma unroll
                for (int mt = 0; mt < 2; ++mt) u[ci][mt] = mfma32(va[mt][ks], wo[ci * 4 + ks], u[ci][mt]);
#pragma unroll
        for (int ci = 0; ci < 4; ++ci)
            st128(rs, OFF_UX + s * MB + (h * 32 + 4 * w + ci) * 1024 + lane * 16,
                  u32x4{pk2(u[ci][0][0], u[ci][0][1]), pk2(u[ci][0][2], u[ci][0][3]), pk2(u[ci][1][0], u[ci][1][1]), pk2(u[ci][1][2], u[ci][1][3])});
        TS(1);
        if (s < 2 || s >= NSEG - 2) { WAIT_VM0(); if (lane == 0) st32(rs, fo, (unsigned)(s + 1)); }
    }
}

constexpr u32x4 POIS4 = {POISON, POISON, POISON, POISON};
DEVI u32x4 san4(u32x4 v) { return u32x4{sanit(v.x), sanit(v.y), sanit(v.z), sanit(v.w)}; }
DEVI void init_done(rsrc_t rs, int tid, int wid) {
    WAIT_VM0();
    __syncthreads();
    if (tid == 0) st32(rs, OFF_FLAG + (FL_INIT + wid - 64) * 4, 1u);
}
DEVI void init_wait(rsrc_t rs, int lane, unsigned& cap) {
    for (unsigned sp = 0;; ++sp) {
        const unsigned fa = ld32(rs, OFF_FLAG + (FL_INIT + lane) * 4), fb = ld32(rs, OFF_FLAG + (FL_INIT + 64 + (lane < 12 ? lane : 0)) * 4);
        MEMBAR();
        if (__all(fa != 0u && fb != 0u)) break;
        if (sp >= cap) { if (cap) st32(rs, OFF_FLAG + FL_ERR * 4, 8u); cap = 0; break; }
        __builtin_amdgcn_s_sleep(16);
    }
}
DEVI void probe_word(rsrc_t rs, int off, unsigned& cap) {
    for (unsigned sp = 0;; ++sp) {
        const unsigned v = ld32(rs, off);
        MEMBAR();
        if (__builtin_amdgcn_readfirstlane(v) != POISON) break;
        if (sp >= cap) { if (cap) st32(rs, OFF_FLAG + FL_ERR * 4, 9u); cap = 0; break; }
        __builtin_amdgcn_s_sleep(2);
    }
}
#define RETRY_OR_DIE(code) if (sp >= cap) { if (cap) st32(rs, OFF_FLAG + FL_ERR * 4, code); cap = 0; break; }
constexpr int TAIL_S = NSEG - 2;

DEVI void tb2_iter(const Params& p, rsrc_t rs, f32x4* red, int tid, int i, int s, u32x4 (&ua)[4][2], u32x4 (&un)[4][2], unsigned& flv, bool& dead) {
    const int lane = tid & 63, w = tid >> 6, g = lane >> 4, l16 = lane & 15;
    char* lds = (char*)red; (void)lds;
    u32x2 xl = {0u, 0u}, xh = {0u, 0u};
    if (w < 2) {
        const __bf16* xs = p.x + (size_t)(32 * s + 16 * w + l16) * 512 + 32 * i + 4 * g;
        xl = *(const u32x2*)xs; xh = *(const u32x2*)(xs + 16);
    }
    {
        const int o = lane < 8 ? (FL_P + 8 * w + lane) : (FL_U + 4 * i + (lane & 3));
        wait_flags(rs, OFF_FLAG + o * 4, true, (unsigned)(s + 1), dead, 1);
    }
    TS(1);
    u32x4 pb[4][2], pm[2], um[2];
#pragma unroll
    for (int mt = 0; mt < 2; ++mt) pm[mt] = ld128(rs, OFF_PM + (s * 32 + 4 * w + g) * 512 + (16 * mt + l16) * 16);
#pragma unroll
    for (int ct = 0; ct < 2; ++ct) um[ct] = ld128(rs, OFF_UM + s * (256 * 1024) + ((4 * w + g) * 512 + 32 * i + 16 * ct + l16) * 16);
#pragma unroll
    for (int hh = 0; hh < 4; ++hh)
#pragma unroll
        for (int mt = 0; mt < 2; ++mt) pb[hh][mt] = ld128(rs, OFF_PX + ((s * 32 + 4 * w + hh) * 2 + mt) * 1024 + lane * 16);
    SCHED0();
    const int fvu = OFF_FLAG + (FL_VU + (i >> 1) * 32 + 4 * w + (lane & 3)) * 4;
    if (s + 2 < NSEG) {
        if (!__all(flv >= (unsigned)(s + 3))) wait_flags(rs, fvu, true, (unsigned)(s + 3), dead, 1);
#pragma unroll
        for (int hh = 0; hh < 4; ++hh)
#pragma unroll
            for (int ct = 0; ct < 2; ++ct) un[hh][ct] = ld128(rs, OFF_UX + (s + 2) * MB + ((4 * w + hh) * 32 + 2 * i + ct) * 1024 + lane * 16);
        flv = ld32(rs, fvu);
    }
    SCHED0();
    f32x4 acc[2][2];
#pragma unroll
    for (int ct = 0; ct < 2; ++ct)
#pragma unroll
        for (int mt = 0; mt < 2; ++mt) acc[ct][mt] = mfma32(um[ct], pm[mt], f32x4{0.f, 0.f, 0.f, 0.f});
#pragma unroll
    for (int hh = 0; hh < 4; ++hh)
#pragma unroll
        for (int ct = 0; ct < 2; ++ct)
#pragma unroll
            for (int mt = 0; mt < 2; ++mt) acc[ct][mt] = mfma32(ua[hh][ct], pb[hh][mt], acc[ct][mt]);
    __syncthreads();
#pragma unroll
    for (int ct = 0; ct < 2; ++ct)
#pragma unroll
        for (int mt = 0; mt < 2; ++mt) red[((w * 2 + ct) * 2 + mt) * 64 + lane] = acc[ct][mt];
    __syncthreads();
    if (w < 2) {
        f32x4 s0 = {0.f, 0.f, 0.f, 0.f}, s1 = s0;
#pragma unroll
        for (int k = 0; k < 8; ++k) { s0 += red[((k * 2 + 0) * 2 + w) * 64 + lane]; s1 += red[((k * 2 + 1) * 2 + w) * 64 + lane]; }
        const u32x4 o = {pk2(bflo(xl.x) + s0[0], bfhi(xl.x) + s0[1]), pk2(bflo(xl.y) + s0[2], bfhi(xl.y) + s0[3]),
                         pk2(bflo(xh.x) + s1[0], bfhi(xh.x) + s1[1]), pk2(bflo(xh.y) + s1[2], bfhi(xh.y) + s1[3])};
        st128(rs, OFF_HF + ((2 * s + w) * 16 + i) * 1024 + lane * 16, san4(o));
    }
    TS(2);
}
DEVI void role_tb2(const Params& p, rsrc_t rs, char* lds, int tid, int par, int i) {
    const int lane = tid & 63, w = tid >> 6;
    f32x4* red = (f32x4*)lds;
    bool dead = false;
#pragma unroll
    for (int k = 0; k < 8; ++k) {
        const int e = w * 8 + k, s = par + 2 * (e >> 1);
        st128(rs, OFF_HF + ((2 * s + (e & 1)) * 16 + i) * 1024 + lane * 16, POIS4);
    }
    init_done(rs, tid, 64 + 2 * i + par);
    u32x4 ua[4][2], ub[4][2];
    const int fvu = OFF_FLAG + (FL_VU + (i >> 1) * 32 + 4 * w + (lane & 3)) * 4;
    wait_flags(rs, fvu, true, (unsigned)(par + 1), dead, 4);
#pragma unroll
    for (int hh = 0; hh < 4; ++hh)
#pragma unroll
        for (int ct = 0; ct < 2; ++ct) ua[hh][ct] = ld128(rs, OFF_UX + par * MB + ((4 * w + hh) * 32 + 2 * i + ct) * 1024 + lane * 16);
    unsigned flv = 0;
    for (int s = par; s < NSEG; s += 4) {
        tb2_iter(p, rs, red, tid, i, s, ua, ub, flv, dead);
        tb2_iter(p, rs, red, tid, i, s + 2, ub, ua, flv, dead);
    }
}

DEVI void unpack8(u32x4 v, float* f) {
    f[0] = bflo(v.x); f[1] = bfhi(v.x); f[2] = bflo(v.y); f[3] = bfhi(v.y); f[4] = bflo(v.z); f[5] = bfhi(v.z); f[6] = bflo(v.w); f[7] = bfhi(v.w);
}
DEVI u32x4 pack8(const float* f) { return u32x4{pk2(f[0], f[1]), pk2(f[2], f[3]), pk2(f[4], f[5]), pk2(f[6], f[7])}; }
DEVI void coop_ln(u32x4 (&fr)[2][2], f32x2* st, int lane, int w) {
    float f[2][16];
#pragma unroll
    for (int mt = 0; mt < 2; ++mt) {
        unpack8(fr[mt][0], f[mt]); unpack8(fr[mt][1], f[mt] + 8);
        float a = 0.f, q = 0.f;
#pragma unroll
        for (int i = 0; i < 16; ++i) { a += f[mt][i]; q += f[mt][i] * f[mt][i]; }
        a = xg_sum(a); q = xg_sum(q);
        if (lane < 16) st[(mt * 16 + lane) * 8 + w] = f32x2{a, q};
    }
    __syncthreads();
#pragma unroll
    for (int mt = 0; mt < 2; ++mt) {
        float a = 0.f, q = 0.f;
        const f32x4* sp = (const f32x4*)(st + (mt * 16 + (lane & 15)) * 8);
#pragma unroll
        for (int i = 0; i < 4; ++i) { const f32x4 v = sp[i]; a += v[0] + v[2]; q += v[1] + v[3]; }
        const float mean = a * (1.0f / 512.0f);
        const float var = fmaxf(q * (1.0f / 512.0f) - mean * mean, 0.f);
        const float rstd = __builtin_amdgcn_rsqf(var + 1e-5f);
#pragma unroll
        for (int i = 0; i < 16; ++i) f[mt][i] = (f[mt][i] - mean) * rstd;
        fr[mt][0] = pack8(f[mt]); fr[mt][1] = pack8(f[mt] + 8);
    }
}

template <int MODE>
DEVI void role_mlp(const Params& p, rsrc_t rs, char* lds, int tid, int par, int i) {
    const int lane = tid & 63, w = tid >> 6, g = lane >> 4, l16 = lane & 15;
    const int pr = w >> 1, mt = w & 1;
    u32x4 wa[2][16];
    {
        const __bf16* wsrc = MODE == 1 ? p.w1 : (MODE == 2 ? p.w2 : p.w3);
        constexpr int LD = MODE == 3 ? 1792 : 512;
        stage_rows<512, 128, false>(wsrc + 128 * i, LD, lds, tid);
        __syncthreads();
#pragma unroll
        for (int nt = 0; nt < 2; ++nt)
#pragma unroll
            for (int ks = 0; ks < 16; ++ks) wa[nt][ks] = tr_frag<128>(lds, ks, 32 * pr + 16 * nt, lane);
        __syncthreads();
    }
    char* zb = lds;
    f32x2* st0 = (f32x2*)(lds + 32768);
    f32x2* st1 = (f32x2*)(lds + 32768 + 2048);
    for (int s = par; s < NSEG; s += 2) {
        if (MODE == 1) {
            st128(rs, OFF_H1F + ((2 * s + mt) * 16 + 4 * i + pr) * 1024 + lane * 16, POIS4);
            st128(rs, OFF_YF + ((2 * s + mt) * 16 + 4 * i + pr) * 1024 + lane * 16, POIS4);
        } else if (MODE == 2) st128(rs, OFF_X1F + ((2 * s + mt) * 16 + 4 * i + pr) * 1024 + lane * 16, POIS4);
        else st128(rs, OFF_AF + ((2 * s + mt) * 56 + 4 * i + pr) * 1024 + lane * 16, POIS4);
    }
    init_done(rs, tid, (MODE == 1 ? 96 : (MODE == 2 ? 104 : 112)) + 2 * i + par);
    unsigned cap = SPIN_CAP;
    init_wait(rs, lane, cap);
    for (int s = par; s < NSEG; s += 2) {
        u32x4 fr[2][2];
        u32x4 yv = {0u, 0u, 0u, 0u};
        if (MODE == 1 || MODE == 3) {
            const int src = (MODE == 1 ? OFF_HF : OFF_X1F) + (2 * s * 16 + 2 * w) * 1024;
            if (s < TAIL_S) probe_word(rs, src + 17 * 1024 + 1020, cap);
            for (unsigned sp = 0;; ++sp) {
#pragma unroll
                for (int m = 0; m < 2; ++m)
#pragma unroll
                    for (int k = 0; k < 2; ++k) fr[m][k] = ld128(rs, src + (m * 16 + k) * 1024 + lane * 16);
                MEMBAR();
                if (__all(umax(umax(mx4(fr[0][0]), mx4(fr[0][1])), umax(mx4(fr[1][0]), mx4(fr[1][1]))) != POISON)) break;
                RETRY_OR_DIE(10u);
            }
            coop_ln(fr, st0, lane, w);
            if (MODE == 1) {
                if ((w >> 1) == i) {
#pragma unroll
                    for (int m = 0; m < 2; ++m)
#pragma unroll
                        for (int k = 0; k < 2; ++k) st128(rs, OFF_YF + ((2 * s + m) * 16 + 2 * w + k) * 1024 + lane * 16, san4(fr[m][k]));
                }
                coop_ln(fr, st1, lane, w);
            }
        } else {
            const int q = w >> 1;
            const int dl = q == 0 ? -1 : (q == 1 ? 1 : (q == 2 ? -2 : 2));
            if (s < TAIL_S) {
                const int T = 32 * s + 31 + dl;
                probe_word(rs, OFF_H1F + ((T >> 4) * 16 + 2 * w + 1) * 1024 + (48 + (T & 15)) * 16 + 12, cap);
            }
            for (unsigned sp = 0;; ++sp) {
                unsigned mxv = 0;
#pragma unroll
                for (int m = 0; m < 2; ++m) {
                    const int T = 32 * s + 16 * m + l16 + dl;
                    const bool ok = T >= 0 && T < 2048;
                    const int Tc = ok ? T : 32 * s;
#pragma unroll
                    for (int k = 0; k < 2; ++k) {
                        u32x4 v = ld128(rs, OFF_H1F + ((Tc >> 4) * 16 + 2 * w + k) * 1024 + (16 * g + (Tc & 15)) * 16);
                        mxv = umax(mxv, mx4(v));
                        fr[m][k] = ok ? v : u32x4{0u, 0u, 0u, 0u};
                    }
                }
                yv = ld128(rs, OFF_YF + ((2 * s + mt) * 16 + 4 * i + pr) * 1024 + lane * 16);
                MEMBAR();
                if (__all(umax(mxv, mx4(yv)) != POISON)) break;
                RETRY_OR_DIE(11u);
            }
        }
        TS(1);
        if (MODE == 2) __syncthreads();
#pragma unroll
        for (int m = 0; m < 2; ++m)
#pragma unroll
            for (int k = 0; k < 2; ++k) *(u32x4*)(zb + (m * 16 + 2 * w + k) * 1024 + lane * 16) = fr[m][k];
        __syncthreads();
        f32x4 a0 = {0.f, 0.f, 0.f, 0.f}, a1 = a0;
        {
            u32x4 fb[3][2];
            const char* zl = zb + mt * 16384 + lane * 16;
#define ML_LD(c) do { fb[(c) % 3][0] = *(const u32x4*)(zl + (2 * (c)) * 1024); fb[(c) % 3][1] = *(const u32x4*)(zl + (2 * (c) + 1) * 1024); } while (0)
            ML_LD(0); ML_LD(1);
#pragma unroll
            for (int c = 0; c < 8; ++c) {
                if (c + 2 < 8) ML_LD(c + 2);
                SCHED0();
                a0 = mfma32(wa[0][2 * c], fb[c % 3][0], a0);
                a1 = mfma32(wa[1][2 * c], fb[c % 3][0], a1);
                a0 = mfma32(wa[0][2 * c + 1], fb[c % 3][1], a0);
                a1 = mfma32(wa[1][2 * c + 1], fb[c % 3][1], a1);
                SCHED0();
            }
        }
        if (MODE == 2) {
            const u32x4 o = {pk2(bflo(yv.x) + rbf(a0[0]), bfhi(yv.x) + rbf(a0[1])), pk2(bflo(yv.y) + rbf(a0[2]), bfhi(yv.y) + rbf(a0[3])),
                             pk2(bflo(yv.z) + rbf(a1[0]), bfhi(yv.z) + rbf(a1[1])), pk2(bflo(yv.w) + rbf(a1[2]), bfhi(yv.w) + rbf(a1[3]))};
            st128(rs, OFF_X1F + ((2 * s + mt) * 16 + 4 * i + pr) * 1024 + lane * 16, san4(o));
        } else {
            const u32x4 o = {pk2(gelu_t(rbf(a0[0])), gelu_t(rbf(a0[1]))), pk2(gelu_t(rbf(a0[2])), gelu_t(rbf(a0[3]))),
                             pk2(gelu_t(rbf(a1[0])), gelu_t(rbf(a1[1]))), pk2(gelu_t(rbf(a1[2])), gelu_t(rbf(a1[3])))};
            if (MODE == 1) st128(rs, OFF_H1F + ((2 * s + mt) * 16 + 4 * i + pr) * 1024 + lane * 16, san4(o));
            else st128(rs, OFF_AF + ((2 * s + mt) * 56 + 4 * i + pr) * 1024 + lane * 16, san4(o));
        }
        TS(2);
    }
}

DEVI void role_m4(const Params& p, rsrc_t rs, char* lds, int tid, int par, int i) {
    const int lane = tid & 63, w = tid >> 6, g = lane >> 4, l16 = lane & 15;
    const int nt = w & 3, kh = w >> 2;
    u32x4 wa[28];
#pragma unroll
    for (int r = 0; r < 2; ++r) {
        stage_rows<896, 64, false>(p.w4 + (size_t)(896 * r) * 512 + 64 * i, 512, lds, tid);
        __syncthreads();
        if (kh == r) {
#pragma unroll
            for (int ks = 0; ks < 28; ++ks) wa[ks] = tr_frag<64>(lds, ks, 16 * nt, lane);
        }
        __syncthreads();
    }
    char* ab = lds;
    f32x4* red = (f32x4*)(lds + 114688);
    unsigned cap = SPIN_CAP;
    init_wait(rs, lane, cap);
    for (int s = par; s < NSEG; s += 2) {
        if (s < TAIL_S) probe_word(rs, OFF_AF + s * 114688 + 13 * 8192 + w * 1024 + 1020, cap);
        u32x4 ar[14];
        for (unsigned sp = 0;; ++sp) {
            unsigned mxv = 0;
#pragma unroll
            for (int k = 0; k < 14; ++k) ar[k] = ld128(rs, OFF_AF + s * 114688 + k * 8192 + tid * 16);
            MEMBAR();
#pragma unroll
            for (int k = 0; k < 14; ++k) mxv = umax(mxv, mx4(ar[k]));
            if (__all(mxv != POISON)) break;
            RETRY_OR_DIE(12u);
        }
        TS(1);
        u32x2 x1v[2], hv[2];
        if (kh == 0) {
#pragma unroll
            for (int m = 0; m < 2; ++m) {
                const int o = ((2 * s + m) * 16 + 2 * i + (nt >> 1)) * 1024 + lane * 16 + (nt & 1) * 8;
                x1v[m] = ld64(rs, OFF_X1F + o); hv[m] = ld64(rs, OFF_HF + o);
            }
        }
        __syncthreads();
#pragma unroll
        for (int k = 0; k < 14; ++k) *(u32x4*)(ab + k * 8192 + tid * 16) = ar[k];
        __syncthreads();
        f32x4 acc[2][2];
#pragma unroll
        for (int m = 0; m < 2; ++m) { acc[m][0] = f32x4{0.f, 0.f, 0.f, 0.f}; acc[m][1] = acc[m][0]; }
#pragma unroll
        for (int ks = 0; ks < 28; ++ks) {
#pragma unroll
            for (int m = 0; m < 2; ++m) {
                const u32x4 b = *(const u32x4*)(ab + (m * 56 + 28 * kh + ks) * 1024 + lane * 16);
                acc[m][ks & 1] = mfma32(wa[ks], b, acc[m][ks & 1]);
            }
        }
        acc[0][0] += acc[0][1]; acc[1][0] += acc[1][1];
        if (kh == 1) { red[(nt * 2 + 0) * 64 + lane] = acc[0][0]; red[(nt * 2 + 1) * 64 + lane] = acc[1][0]; }
        __syncthreads();
        if (kh == 0) {
#pragma unroll
            for (int m = 0; m < 2; ++m) {
                const f32x4 d = acc[m][0] + red[(nt * 2 + m) * 64 + lane];
                const float t0 = rbf(bflo(x1v[m].x) + rbf(d[0])), t1 = rbf(bfhi(x1v[m].x) + rbf(d[1]));
                const float t2 = rbf(bflo(x1v[m].y) + rbf(d[2])), t3 = rbf(bfhi(x1v[m].y) + rbf(d[3]));
                const u32x2 o = {pk2(bflo(hv[m].x) + t0, bfhi(hv[m].x) + t1), pk2(bflo(hv[m].y) + t2, bfhi(hv[m].y) + t3)};
                *(u32x2*)(p.out + (size_t)(32 * s + 16 * m + l16) * 512 + 64 * i + 16 * nt + 4 * g) = o;
            }
        }
        TS(2);
    }
}

__global__ void __launch_bounds__(512, 2) main_kernel(Params p) {
    extern __shared__ __attribute__((aligned(16))) char lds[];
    const int tid = threadIdx.x, b = blockIdx.x;
    const rsrc_t rs = __builtin_amdgcn_make_buffer_rsrc(p.ws, 0, WS_SPAN, 0x00020000);
    const int xcd = b & 7, slot = b >> 3;
    if (xcd == 2) role_score(p, rs, lds, tid, slot);
    else if (xcd < 2) role_col(p, rs, lds, tid, slot * 2 + xcd);
    else {
        const int wid = slot * 5 + (xcd - 3);
        if (wid == 155) role_probe(rs, lds, tid);
        else if (wid < 32) role_qk(p, rs, lds, tid, wid);
        else if (wid < 64) role_vu(p, rs, lds, tid, wid - 32);
        else if (wid < 96) role_tb2(p, rs, lds, tid, (wid - 64) & 1, (wid - 64) >> 1);
        else if (wid < 104) role_mlp<1>(p, rs, lds, tid, (wid - 96) & 1, (wid - 96) >> 1);
        else if (wid < 112) role_mlp<2>(p, rs, lds, tid, (wid - 104) & 1, (wid - 104) >> 1);
        else if (wid < 140) role_mlp<3>(p, rs, lds, tid, (wid - 112) & 1, (wid - 112) >> 1);
        else if (wid < 155) role_m4(p, rs, lds, tid, (wid - 140) & 1, (wid - 140) >> 1);
        else if (wid == 156) role_m4(p, rs, lds, tid, 1, 7);
    }
}

extern "C" void kernel_launch(void* const* d_in, const int* in_sizes, int n_in, void* d_out, int out_size, void* d_ws, size_t ws_size,
                              hipStream_t stream) {
    (void)in_sizes; (void)n_in; (void)out_size; (void)ws_size;
    Params p;
    p.x = (const __bf16*)d_in[0]; p.mem0 = (const __bf16*)d_in[1];
    p.wq = (const __bf16*)d_in[2]; p.wk = (const __bf16*)d_in[3]; p.wv = (const __bf16*)d_in[4]; p.wo = (const __bf16*)d_in[5];
    p.w1 = (const __bf16*)d_in[8]; p.w2 = (const __bf16*)d_in[9]; p.w3 = (const __bf16*)d_in[10]; p.w4 = (const __bf16*)d_in[11];
    p.out = (__bf16*)d_out;
    p.ws = (char*)(((uintptr_t)d_ws + 262143u) & ~(uintptr_t)262143u);
    hipFuncSetAttribute((const void*)prep_kernel, hipFuncAttributeMaxDynamicSharedMemorySize, LDS_BYTES);
    hipFuncSetAttribute((const void*)main_kernel, hipFuncAttributeMaxDynamicSharedMemorySize, LDS_BYTES);
    hipLaunchKernelGGL(prep_kernel, dim3(256), dim3(512), LDS_BYTES, stream, p);
    hipLaunchKernelGGL(main_kernel, dim3(256), dim3(512), LDS_BYTES, stream, p);
}
```

```cpp
#include <hip/hip_runtime.h>
#include <hip/hip_bf16.h>
#include <stdint.h>

typedef __bf16 bf16x2 __attribute__((ext_vector_type(2)));
typedef __bf16 bf16x4 __attribute__((ext_vector_type(4)));
typedef __bf16 bf16x8 __attribute__((ext_vector_type(8)));
typedef __bf16 bf16x16 __attribute__((ext_vector_type(16)));
typedef short s16x4 __attribute__((ext_vector_type(4)));
typedef float f32x2 __attribute__((ext_vector_type(2)));
typedef float f32x4 __attribute__((ext_vector_type(4)));
typedef unsigned u32x2 __attribute__((ext_vector_type(2)));
typedef unsigned u32x4 __attribute__((ext_vector_type(4)));
typedef unsigned u32x8 __attribute__((ext_vector_type(8)));
typedef __attribute__((address_space(3))) s16x4* lds_s16x4_ptr;
typedef __amdgpu_buffer_rsrc_t rsrc_t;

#define DEVI __device__ __forceinline__
#define MEMBAR() asm volatile("" ::: "memory")
#define WAIT_VM0() asm volatile("s_waitcnt vmcnt(0)" ::: "memory")

constexpr int NSEG = 64;
constexpr int SC1 = 16;
constexpr unsigned SPIN_CAP = 400000u;
constexpr unsigned POISON = 0xFFFFFFFFu;
constexpr int OOB = 0x7FFFFF00;

constexpr int MB = 1 << 20;
constexpr int OFF_FLAG = 256 * 1024;
constexpr int OFF_XNF = 1 * MB;
constexpr int OFF_HF = 3 * MB;
constexpr int OFF_YF = 5 * MB;
constexpr int OFF_H1F = 7 * MB;
constexpr int OFF_X1F = 9 * MB;
constexpr int OFF_AF = 11 * MB;
constexpr int OFF_PX = 18 * MB;
constexpr int OFF_PM = 22 * MB;
constexpr int OFF_BIMG = 24 * MB;
constexpr int OFF_QX = 40 * MB;
constexpr int OFF_KX = 56 * MB;
constexpr int OFF_UM = 72 * MB;
constexpr int OFF_UX = 88 * MB;
constexpr int OFF_DBG = 152 * MB;
constexpr int OFF_HA = 160 * MB;
constexpr int OFF_HB = 162 * MB;
constexpr int WS_SPAN = 164 * MB;
constexpr int FL_SEL = 0;
constexpr int FL_ERR = 32;
constexpr int FL_INIT = 64;
constexpr int FL_QK = 256;
constexpr int FL_VU = 256 + 1024;
constexpr int FL_P = 256 + 2048;
constexpr int FL_U = 256 + 3072;
constexpr int FL_WORDS = 256 + 3072 + 2048;

constexpr int LDS_BYTES = 151552;

struct Params {
    const __bf16* x; const __bf16* mem0; const __bf16* wq; const __bf16* wk; const __bf16* wv; const __bf16* wo;
    const __bf16* w1; const __bf16* w2; const __bf16* w3; const __bf16* w4;
    __bf16* out; char* ws;
};

DEVI unsigned pk2(float a, float b) {
    f32x2 v = {a, b};
    bf16x2 r = __builtin_convertvector(v, bf16x2);
    return __builtin_bit_cast(unsigned, r);
}
DEVI float bflo(unsigned u) { return __builtin_bit_cast(float, u << 16); }
DEVI float bfhi(unsigned u) { return __builtin_bit_cast(float, u & 0xFFFF0000u); }
DEVI float rbf(float a) { return bflo(pk2(a, 0.f)); }
DEVI f32x4 mfma32(u32x4 a, u32x4 b, f32x4 c) {
    return __builtin_amdgcn_mfma_f32_16x16x32_bf16(__builtin_bit_cast(bf16x8, a), __builtin_bit_cast(bf16x8, b), c, 0, 0, 0);
}
DEVI f32x4 mfma16(u32x2 a, u32x2 b, f32x4 c) {
    return __builtin_amdgcn_mfma_f32_16x16x16bf16_1k(__builtin_bit_cast(s16x4, a), __builtin_bit_cast(s16x4, b), c, 0, 0, 0);
}
DEVI f32x4 smfmac(u32x4 a, u32x4 b0, u32x4 b1, f32x4 c, int idx) {
    u32x8 b = __builtin_shufflevector(b0, b1, 0, 1, 2, 3, 4, 5, 6, 7);
    return __builtin_amdgcn_smfmac_f32_16x16x64_bf16(__builtin_bit_cast(bf16x8, a), __builtin_bit_cast(bf16x16, b), c, idx, 0, 0);
}
DEVI u32x4 ld128(rsrc_t rs, int off) { return __builtin_amdgcn_raw_buffer_load_b128(rs, off, 0, SC1); }
DEVI u32x2 ld64(rsrc_t rs, int off) { return __builtin_amdgcn_raw_buffer_load_b64(rs, off, 0, SC1); }
DEVI unsigned ld32(rsrc_t rs, int off) { return __builtin_amdgcn_raw_buffer_load_b32(rs, off, 0, SC1); }
DEVI void st128(rsrc_t rs, int off, u32x4 v) { __builtin_amdgcn_raw_buffer_store_b128(v, rs, off, 0, SC1); }
DEVI void st64(rsrc_t rs, int off, u32x2 v) { __builtin_amdgcn_raw_buffer_store_b64(v, rs, off, 0, SC1); }
DEVI void st32(rsrc_t rs, int off, unsigned v) { __builtin_amdgcn_raw_buffer_store_b32(v, rs, off, 0, SC1); }
DEVI int amap(int x) { return (x & 8191) | ((x >> 13) << 14); }
DEVI int ho_mem(int sel, int s) { return OFF_HB + s * 16384 + (((sel >> 1) & 1) << 13); }
DEVI int ho_px(int sel, int s) { return OFF_HA + s * 32768 + ((sel & 1) << 13); }
DEVI int ho_pm(int sel, int s) { return OFF_HB + MB + (s >> 1) * 16384 + (s & 1) * 4096 + (((sel >> 1) & 1) << 13); }
DEVI unsigned sanit(unsigned u) { return u < 0xFFFFFFFEu ? u : 0xFFFFFFFEu; }

template <int CTRL> DEVI float dppf(float v) {
    return __builtin_bit_cast(float, __builtin_amdgcn_update_dpp(0, __builtin_bit_cast(int, v), CTRL, 0xF, 0xF, true));
}
template <int CTRL> DEVI unsigned dppu(unsigned v) {
    return (unsigned)__builtin_amdgcn_update_dpp(0, (int)v, CTRL, 0xF, 0xF, true);
}
constexpr int DPP_ROR1 = 0x121, DPP_ROR2 = 0x122, DPP_ROR4 = 0x124, DPP_ROR8 = 0x128;
constexpr int DPP_SHL2 = 0x102, DPP_SHL4 = 0x104, DPP_SHL6 = 0x106;
constexpr int DPP_XOR1 = 0xB1;

DEVI void swap32(unsigned& a, unsigned& b) { auto r = __builtin_amdgcn_permlane32_swap(a, b, false, false); a = r[0]; b = r[1]; }
DEVI void swap16(unsigned& a, unsigned& b) { auto r = __builtin_amdgcn_permlane16_swap(a, b, false, false); a = r[0]; b = r[1]; }
DEVI float fold32(float x, float y) {
    unsigned a = __builtin_bit_cast(unsigned, x), b = __builtin_bit_cast(unsigned, y);
    swap32(a, b);
    return __builtin_bit_cast(float, a) + __builtin_bit_cast(float, b);
}
DEVI float xg_sum(float v) {
    unsigned a = __builtin_bit_cast(unsigned, v), b = a; swap16(a, b);
    v = __builtin_bit_cast(float, a) + __builtin_bit_cast(float, b);
    a = __builtin_bit_cast(unsigned, v); b = a; swap32(a, b);
    return __builtin_bit_cast(float, a) + __builtin_bit_cast(float, b);
}
DEVI float xg_max(float v) {
    unsigned a = __builtin_bit_cast(unsigned, v), b = a; swap16(a, b);
    v = fmaxf(__builtin_bit_cast(float, a), __builtin_bit_cast(float, b));
    a = __builtin_bit_cast(unsigned, v); b = a; swap32(a, b);
    return fmaxf(__builtin_bit_cast(float, a), __builtin_bit_cast(float, b));
}
DEVI float wave_sum(float v) {
    v += dppf<DPP_ROR1>(v); v += dppf<DPP_ROR2>(v); v += dppf<DPP_ROR4>(v); v += dppf<DPP_ROR8>(v);
    return xg_sum(v);
}
DEVI float wave_max(float v) {
    v = fmaxf(v, dppf<DPP_ROR1>(v)); v = fmaxf(v, dppf<DPP_ROR2>(v)); v = fmaxf(v, dppf<DPP_ROR4>(v)); v = fmaxf(v, dppf<DPP_ROR8>(v));
    return xg_max(v);
}
DEVI float wave_max_u(float v) {
    float t;
    asm volatile("s_nop 1\n\t"
                 "v_max_f32_dpp %0, %1, %1 row_ror:8 row_mask:0xf bank_mask:0xf\n\ts_nop 1\n\t"
                 "v_max_f32_dpp %0, %0, %0 row_ror:4 row_mask:0xf bank_mask:0xf\n\ts_nop 1\n\t"
                 "v_max_f32_dpp %0, %0, %0 row_ror:2 row_mask:0xf bank_mask:0xf\n\ts_nop 1\n\t"
                 "v_max_f32_dpp %0, %0, %0 row_ror:1 row_mask:0xf bank_mask:0xf\n\ts_nop 1\n\t"
                 "v_max_f32_dpp %0, %0, %0 row_bcast:15 row_mask:0xa bank_mask:0xf\n\ts_nop 1\n\t"
                 "v_max_f32_dpp %0, %0, %0 row_bcast:31 row_mask:0xc bank_mask:0xf\n\ts_nop 1"
                 : "=&v"(t) : "v"(v));
    return __builtin_bit_cast(float, __builtin_amdgcn_readlane(__builtin_bit_cast(int, t), 63));
}
DEVI float wave_sum_u(float v) {
    float t;
    asm volatile("s_nop 1\n\t"
                 "v_add_f32_dpp %0, %1, %1 row_ror:8 row_mask:0xf bank_mask:0xf\n\ts_nop 1\n\t"
                 "v_add_f32_dpp %0, %0, %0 row_ror:4 row_mask:0xf bank_mask:0xf\n\ts_nop 1\n\t"
                 "v_add_f32_dpp %0, %0, %0 row_ror:2 row_mask:0xf bank_mask:0xf\n\ts_nop 1\n\t"
                 "v_add_f32_dpp %0, %0, %0 row_ror:1 row_mask:0xf bank_mask:0xf\n\ts_nop 1\n\t"
                 "v_add_f32_dpp %0, %0, %0 row_bcast:15 row_mask:0xa bank_mask:0xf\n\ts_nop 1\n\t"
                 "v_add_f32_dpp %0, %0, %0 row_bcast:31 row_mask:0xc bank_mask:0xf\n\ts_nop 1"
                 : "=&v"(t) : "v"(v));
    return __builtin_bit_cast(float, __builtin_amdgcn_readlane(__builtin_bit_cast(int, t), 63));
}
DEVI u32x2 trd(const char* p) { s16x4 a = __builtin_amdgcn_ds_read_tr16_b64_v4i16((lds_s16x4_ptr)p); return __builtin_bit_cast(u32x2, a); }
DEVI float vmax2(float a, float b) { return __builtin_amdgcn_fmed3f(a, b, INFINITY); }
DEVI float vmax3(float a, float b, float c) { float r; asm("v_max3_f32 %0, %1, %2, %3" : "=v"(r) : "v"(a), "v"(b), "v"(c)); return r; }
DEVI float q8_max(float v) {
    float t, u;
    asm volatile("s_nop 1\n\t"
                 "v_max_f32_dpp %0, %2, %2 row_ror:8 row_mask:0xf bank_mask:0xf\n\t"
                 "v_mov_b32 %1, %0\n\ts_nop 1\n\t"
                 "v_permlane16_swap_b32 %0, %1\n\t"
                 "v_max_f32 %0, %0, %1\n\t"
                 "v_mov_b32 %1, %0\n\ts_nop 1\n\t"
                 "v_permlane32_swap_b32 %0, %1\n\t"
                 "v_max_f32 %0, %0, %1"
                 : "=&v"(t), "=&v"(u) : "v"(v));
    return t;
}
DEVI float q8_sum(float v) {
    float t, u;
    asm volatile("s_nop 1\n\t"
                 "v_add_f32_dpp %0, %2, %2 row_ror:8 row_mask:0xf bank_mask:0xf\n\t"
                 "v_mov_b32 %1, %0\n\ts_nop 1\n\t"
                 "v_permlane16_swap_b32 %0, %1\n\t"
                 "v_add_f32 %0, %0, %1\n\t"
                 "v_mov_b32 %1, %0\n\ts_nop 1\n\t"
                 "v_permlane32_swap_b32 %0, %1\n\t"
                 "v_add_f32 %0, %0, %1"
                 : "=&v"(t), "=&v"(u) : "v"(v));
    return t;
}
DEVI unsigned now32() { return (unsigned)__builtin_amdgcn_s_memtime(); }
DEVI void nap_until(unsigned t) { while ((int)(now32() - t) < 0) __builtin_amdgcn_s_sleep(1); }
#define SCHED0() __builtin_amdgcn_sched_barrier(0)
DEVI unsigned umax(unsigned a, unsigned b) { return a > b ? a : b; }
DEVI float gelu_t(float x) {
    const float u = x * (0.7978845608028654f + 0.0356774081363001f * x * x);
    const float e = __builtin_amdgcn_exp2f(-2.885390081777927f * u);
    return x * __builtin_amdgcn_rcpf(1.0f + e);
}

DEVI void wait_flags(rsrc_t rs, int off, bool act, unsigned v, bool& dead, int slp) {
    if (dead) return;
    for (unsigned sp = 0;; ++sp) {
        unsigned f = ld32(rs, off);
        MEMBAR();
        if (__all(!act || f >= v)) break;
        if (sp > SPIN_CAP) { dead = true; st32(rs, OFF_FLAG + FL_ERR * 4, 1u); break; }
        for (int i = 0; i < slp; ++i) __builtin_amdgcn_s_sleep(2);
    }
}

#define ROT 1
#define ROT_S(h) ((h) >> 1)
#define ROT_Q(h) (((h) >> 1) + 8)
template <int NROWS, int NCOLS>
DEVI void stage_load(const __bf16* __restrict__ src, int ld, int tid, u32x4 (&v)[NROWS * NCOLS / 4096], int rot) {
    constexpr int CPR = NCOLS / 8;
#pragma unroll
    for (int i = 0; i < NROWS * NCOLS / 4096; ++i) {
        const int c = tid + 512 * ((i + rot) & 15), row = c / CPR, cc = c % CPR;
        v[i] = *(const u32x4*)(src + (size_t)row * ld + cc * 8);
    }
}
template <int NROWS, int NCOLS, bool PERM>
DEVI void stage_store(const u32x4 (&v)[NROWS * NCOLS / 4096], char* lds, int tid, int rot) {
    constexpr int CPR = NCOLS / 8;
    constexpr int PITCH = NCOLS * 2 + 32;
#pragma unroll
    for (int i = 0; i < NROWS * NCOLS / 4096; ++i) {
        const int c = tid + 512 * ((i + rot) & 15), row = c / CPR, cc = c % CPR;
        int lrow = row;
        if (PERM) {
            const int d = row & 31;
            const int g = 2 * (d >> 4) + ((d >> 2) & 1), r = 2 * ((d >> 3) & 1) + (d & 1), hf = (d >> 1) & 1;
            lrow = (row & ~31) + 16 * hf + 4 * g + r;
        }
        *(u32x4*)(lds + lrow * PITCH + cc * 16) = v[i];
    }
}
template <int NROWS, int NCOLS, bool PERM>
DEVI void stage_rows(const __bf16* __restrict__ src, int ld, char* lds, int tid) {
    constexpr int CPR = NCOLS / 8;
    constexpr int TOT = NROWS * CPR;
    constexpr int PER = TOT / 512;
    constexpr int PITCH = NCOLS * 2 + 32;
    u32x4 v[PER];
#pragma unroll
    for (int i = 0; i < PER; ++i) {
        const int c = tid + 512 * i, row = c / CPR, cc = c % CPR;
        v[i] = *(const u32x4*)(src + (size_t)row * ld + cc * 8);
    }
#pragma unroll
    for (int i = 0; i < PER; ++i) {
        const int c = tid + 512 * i, row = c / CPR, cc = c % CPR;
        int lrow = row;
        if (PERM) {
            const int d = row & 31;
            const int g = 2 * (d >> 4) + ((d >> 2) & 1), r = 2 * ((d >> 3) & 1) + (d & 1), hf = (d >> 1) & 1;
            lrow = (row & ~31) + 16 * hf + 4 * g + r;
        }
        *(u32x4*)(lds + lrow * PITCH + cc * 16) = v[i];
    }
}
template <int NCOLS>
DEVI u32x4 tr_frag(const char* lds, int ks, int col0, int lane) {
    constexpr int PITCH = NCOLS * 2 + 32;
    const int g = lane >> 4, i = lane & 15;
    const char* p = lds + (32 * ks + 4 * g + (i >> 2)) * PITCH + (col0 + 4 * (i & 3)) * 2;
    s16x4 a = __builtin_amdgcn_ds_read_tr16_b64_v4i16((lds_s16x4_ptr)p);
    s16x4 b = __builtin_amdgcn_ds_read_tr16_b64_v4i16((lds_s16x4_ptr)(p + 16 * PITCH));
    u32x2 ua = __builtin_bit_cast(u32x2, a), ub = __builtin_bit_cast(u32x2, b);
    return u32x4{ua.x, ua.y, ub.x, ub.y};
}

__global__ void __launch_bounds__(512, 2) prep_kernel(Params p) {
    extern __shared__ __attribute__((aligned(16))) char lds[];
    const int tid = threadIdx.x, lane = tid & 63, w = tid >> 6, b = blockIdx.x;
    const int h = b & 31, t = b >> 5;
    char* ws = p.ws;
#pragma unroll
    for (int k = 0; k < 2; ++k) {
        const int phys = ((k * 256 + b) * 512 + tid) * 16;
        u32x4 v = {POISON, POISON, POISON, POISON};
        if (phys >= 2 * MB && phys < 2 * MB + 16384) {
            const int o = phys & 8191, j = o >> 7, r = (o >> 4) & 7;
            v = *(const u32x4*)(p.mem0 + r * 512 + 8 * j);
            v.x = sanit(v.x); v.y = sanit(v.y); v.z = sanit(v.z); v.w = sanit(v.w);
        }
        if (phys < 3 * MB + MB / 2) *(u32x4*)(ws + OFF_HA + phys) = v;
    }
    if (b >= 32 && b < 48) {
        const int i = (b - 32) * 512 + tid;
        if (i < FL_WORDS) *(unsigned*)(ws + OFF_FLAG + i * 4) = 0u;
    }
    stage_rows<128, 512, false>(p.wo + (size_t)h * 128 * 512, 512, lds, tid);
    u32x4 av[4][4];
    {
        const int g = lane >> 4;
#pragma unroll
        for (int dt = 0; dt < 4; ++dt)
#pragma unroll
            for (int ks = 0; ks < 4; ++ks) {
                const __bf16* src = p.wv + (size_t)(64 * t + 16 * dt + (lane & 15)) * 4096 + h * 128 + 32 * ks + 4 * g;
                u32x2 lo = *(const u32x2*)src, hi = *(const u32x2*)(src + 16);
                av[dt][ks] = u32x4{lo.x, lo.y, hi.x, hi.y};
            }
    }
    {
        const int T = 8 * b + w, ks = lane >> 2, g = lane & 3;
        const __bf16* src = p.x + (size_t)T * 512 + 32 * ks + 4 * g;
        u32x2 lo = *(const u32x2*)src, hi = *(const u32x2*)(src + 16);
        float f[8] = {bflo(lo.x), bfhi(lo.x), bflo(lo.y), bfhi(lo.y), bflo(hi.x), bfhi(hi.x), bflo(hi.y), bfhi(hi.y)};
        float s = 0.f;
#pragma unroll
        for (int i = 0; i < 8; ++i) s += f[i];
        const float mean = wave_sum(s) * (1.0f / 512.0f);
        float q = 0.f;
#pragma unroll
        for (int i = 0; i < 8; ++i) { f[i] -= mean; q += f[i] * f[i]; }
        const float rstd = __builtin_amdgcn_rsqf(wave_sum(q) * (1.0f / 512.0f) + 1e-5f);
        u32x4 o = {pk2(f[0] * rstd, f[1] * rstd), pk2(f[2] * rstd, f[3] * rstd), pk2(f[4] * rstd, f[5] * rstd), pk2(f[6] * rstd, f[7] * rstd)};
        *(u32x4*)(ws + OFF_XNF + ((T >> 4) * 16 + ks) * 1024 + (16 * g + (T & 15)) * 16) = o;
    }
    __syncthreads();
#pragma unroll
    for (int ci = 0; ci < 4; ++ci) {
        const int ct = 4 * w + ci;
        u32x4 bw[4];
#pragma unroll
        for (int ks = 0; ks < 4; ++ks) bw[ks] = tr_frag<512>(lds, ks, 16 * ct, lane);
        f32x4 acc[4];
#pragma unroll
        for (int dt = 0; dt < 4; ++dt) {
            acc[dt] = f32x4{0.f, 0.f, 0.f, 0.f};
#pragma unroll
            for (int ks = 0; ks < 4; ++ks) acc[dt] = mfma32(av[dt][ks], bw[ks], acc[dt]);
        }
#pragma unroll
        for (int half = 0; half < 2; ++half) {
            unsigned x0 = pk2(acc[2 * half][0], acc[2 * half][1]), x1 = pk2(acc[2 * half][2], acc[2 * half][3]);
            unsigned y0 = pk2(acc[2 * half + 1][0], acc[2 * half + 1][1]), y1 = pk2(acc[2 * half + 1][2], acc[2 * half + 1][3]);
            swap32(x0, y0); swap32(x1, y1);
            const int c16 = lane & 15, bb = (lane >> 4) & 1, tile = lane >> 5;
            const int gB = 2 * tile + bb, j = 2 * ct + (c16 >> 3), u = c16 & 7;
            const int n = 8 * (h & 1) + u;
            const size_t off = (((((size_t)j * 8 + (h >> 2)) * 2 + ((h >> 1) & 1)) * 8 + t) * 2 + half) * 1024 + (16 * gB + n) * 16;
            *(u32x4*)(ws + OFF_BIMG + off) = u32x4{x0, y0, x1, y1};
        }
    }
}

#define PROBE_N 10
DEVI void role_probe(rsrc_t rs, char* lds, int tid) {
    const int lane = tid & 63, w = tid >> 6;
    unsigned* t = (unsigned*)lds;
    {
        unsigned sum[2] = {0u, 0u};
        const int po = (w < 4 ? OFF_HA + 63 * 32768 : OFF_HB + MB + MB / 2) + (w & 3) * 1024;
        for (int rep = 0; rep < 2 * PROBE_N; ++rep) {
            const unsigned long long c1 = __builtin_amdgcn_s_memtime();
            st32(rs, lane == 0 ? po + (rep & 1) * 8192 + ((rep >> 1) & 7) * 128 : OOB, (unsigned)rep);
            WAIT_VM0();
            const unsigned long long c2 = __builtin_amdgcn_s_memtime();
            const unsigned d = (unsigned)(c2 - c1);
            sum[rep & 1] += d < 1200u ? d : 1200u;
        }
        if (lane == 0) { t[2 * w] = sum[0]; t[2 * w + 1] = sum[1]; }
    }
    __syncthreads();
    if (tid == 0) {
        const unsigned xcc = __builtin_amdgcn_s_getreg((20 << 0) | (0 << 6) | (3 << 11)) & 15u;
        const unsigned a0 = t[0] + t[2] + t[4] + t[6], a1 = t[1] + t[3] + t[5] + t[7], b0 = t[8] + t[10] + t[12] + t[14], b1 = t[9] + t[11] + t[13] + t[15];
        unsigned near1 = (a1 < a0 ? 1u : 0u) | (b1 < b0 ? 2u : 0u);
        if (xcc >= 4) near1 ^= 3u;
        st32(rs, OFF_FLAG + FL_SEL * 4, 1u + near1);
    }
}

DEVI int get_base(rsrc_t rs, bool& dead) {
    unsigned f = 0;
    for (unsigned sp = 0;; ++sp) {
        f = ld32(rs, OFF_FLAG + FL_SEL * 4);
        MEMBAR();
        f = __builtin_amdgcn_readfirstlane(f);
        if (f != 0) break;
        if (sp > SPIN_CAP) { dead = true; st32(rs, OFF_FLAG + FL_ERR * 4, 2u); break; }
        __builtin_amdgcn_s_sleep(4);
    }
    return (int)((f - 1u) & 3u);
}
DEVI unsigned mx4(u32x4 v) { return umax(umax(v.x, v.y), umax(v.z, v.w)); }
DEVI bool okw(u32x4 v) { return mx4(v) != POISON; }
constexpr int HO_PSTR = 24576;

#define DLY_S 3000
#define DLY_P 3250
#define DLY_C2 750
#define FL_EARLY 2
#define FL_LEAD 100
#define THROTTLE(s) do { } while (0)
#define CNT(i) do { } while (0)
#define CNT_DECL do { } while (0)
#define CNT_OUT() do { } while (0)
#define TS(ev) do { } while (0)
#define TSC(ev, mask) do { } while (0)
DEVI void nap(int n) { for (int i = 0; i < n; ++i) __builtin_amdgcn_s_sleep(1); }

DEVI void role_score(const Params& p, rsrc_t rs, char* lds, int tid, int h) {
    const int rot = ROT_S(h);
    const int lane = tid & 63, w = __builtin_amdgcn_readfirstlane(tid >> 6), g = lane >> 4, l16 = lane & 15;
    u32x4 wq[16], wk[16];
    {
        u32x4 v1[16], v2[16];
        stage_load<512, 128>(p.wq + h * 128, 4096, tid, v1, rot);
        stage_load<512, 128>(p.wk + h * 128, 4096, tid, v2, rot);
        SCHED0();
        stage_store<512, 128, true>(v1, lds, tid, rot);
        __syncthreads();
#pragma unroll
        for (int ks = 0; ks < 16; ++ks) wq[ks] = tr_frag<128>(lds, ks, 16 * w, lane);
        __syncthreads();
        stage_store<512, 128, true>(v2, lds, tid, rot);
    }
    __syncthreads();
#pragma unroll
    for (int ks = 0; ks < 16; ++ks) wk[ks] = tr_frag<128>(lds, ks, 16 * w, lane);
    __syncthreads();
    char* membuf = lds;
    char* kbuf = lds + 8192;
    char* qbuf = lds + 8192 + 16384;
    char* img = lds + 8192 + 32768;
    for (int i = tid; i < 128 * 20; i += 512) ((unsigned*)img)[i] = 0u;
    const int idx = l16 < 8 ? 0x4444 : 0xEEEE;
    bool dead = false;
    const int base = get_base(rs, dead);
    const int fqk = OFF_FLAG + (FL_QK + h * 32) * 4;
    wait_flags(rs, fqk, true, 1u, dead, 2);
    unsigned cap = dead ? 0u : SPIN_CAP;
    u32x4 kx = ld128(rs, OFF_KX + (h * 8 + w) * 1024 + lane * 16);
    u32x4 qx = ld128(rs, OFF_QX + (h * 8 + w) * 1024 + lane * 16);
    unsigned fl = ld32(rs, fqk), avail = 1u;
    const float sc2 = 0.08838834764831845f * 1.4426950408889634f;
    const int pmo = w * 1024 + lane * 16;
    const float lmask = l16 < 8 ? 0.f : -INFINITY;
    char* yw = img + (16 * w + l16) * 80 + (g < 2 ? 16 + 4 * g : 48 + 16 * (g - 2));
    const char* yr = img + (4 * g + (l16 >> 2)) * 80 + (l16 & 3) * 8;
    const int qn = (l16 & 1) | (((l16 >> 1) & 1) << 2) | (((l16 >> 2) & 1) << 1);
    const int pxo = amap((h >> 1) * 1024 + g * 256 + (h & 1) * 128 + qn * 16 + (l16 >> 3) * 8);
    const int pmm = h * 128 + qn * 16 + g * 4;
    const int kqw = (w >> 1) * 1024 + lane * 16 + (w & 1) * 8;
    unsigned tpoll = now32();
    CNT_DECL;
    for (int s = 0; s < NSEG; ++s) {
        const int par = s & 1;
        *(u32x2*)(kbuf + par * 8192 + kqw) = u32x2{kx.x, kx.y}; *(u32x2*)(kbuf + par * 8192 + 4096 + kqw) = u32x2{kx.z, kx.w};
        *(u32x2*)(qbuf + par * 8192 + kqw) = u32x2{qx.x, qx.y}; *(u32x2*)(qbuf + par * 8192 + 4096 + kqw) = u32x2{qx.z, qx.w};
        avail = umax(avail, __builtin_amdgcn_readfirstlane(fl));
        int ax = ho_px(base, s) + pxo, am = l16 < 8 ? ho_pm(base, s) + pmm : OOB, off = ho_mem(base, s) + pmo;
        asm volatile("" : "+v"(ax), "+v"(am), "+v"(off));
        __syncthreads();
        u32x4 kf[8];
        if (w == 2) {
#pragma unroll
            for (int t = 0; t < 8; ++t) kf[t] = *(const u32x4*)(kbuf + par * 8192 + t * 1024 + lane * 16);
#pragma unroll
            for (int t = 0; t < 8; ++t) asm volatile("" : "+v"(kf[t]));
        }
        nap_until(tpoll);
        u32x4 v;
        {
            unsigned sp = 0; bool ok;
            do { v = ld128(rs, off); *(u32x4*)(membuf + w * 1024 + lane * 16) = v; MEMBAR(); ok = __all(okw(v)); ++sp; } while (!ok && sp < cap);
            CNT(0);
            TSC(0, TSM_S);
            if (__builtin_expect(!ok, 0)) { if (cap) st32(rs, OFF_FLAG + FL_ERR * 4, 3u); cap = 0; }
        }
        __syncthreads();
        TSC(1, TSM_S);
        u32x4 a[8];
#pragma unroll
        for (int t = 0; t < 8; ++t) a[t] = *(const u32x4*)(membuf + t * 1024 + lane * 16);
        SCHED0();
        f32x4 aq = {0.f, 0.f, 0.f, 0.f}, ak = {0.f, 0.f, 0.f, 0.f};
#pragma unroll
        for (int t = 0; t < 8; ++t) {
            aq = smfmac(a[t], wq[2 * t], wq[2 * t + 1], aq, idx);
            ak = smfmac(a[t], wk[2 * t], wk[2 * t + 1], ak, idx);
        }
        SCHED0();
        tpoll = now32() + DLY_S;
        f32x4 X;
#pragma unroll
        for (int i = 0; i < 4; ++i) X[i] = fold32(aq[i], ak[i]);
        *(unsigned*)yw = pk2(X[0], X[1]);
        *(unsigned*)(yw + 8) = pk2(X[2], X[3]);
        TSC(2, TSM_S);
        __syncthreads();
        TSC(3, TSM_S);
        if (__builtin_expect(w == 2 && s < NSEG - 1, 1)) {
            u32x2 y[8], km[8];
#pragma unroll
            for (int c = 0; c < 4; ++c) {
                y[2 * c] = trd(yr + (2 * c) * 1280 + 16); y[2 * c + 1] = trd(yr + (2 * c + 1) * 1280 + 16);
                km[2 * c] = trd(yr + (2 * c) * 1280 + 48); km[2 * c + 1] = trd(yr + (2 * c + 1) * 1280 + 48);
            }
            SCHED0();
            f32x4 D0 = {0.f, 0.f, 0.f, 0.f}, D1 = D0, Dm = D0;
#pragma unroll
            for (int c = 0; c < 4; ++c) {
                const u32x4 B = {y[2 * c].x, y[2 * c].y, y[2 * c + 1].x, y[2 * c + 1].y};
                D0 = mfma32(kf[c], B, D0);
                D1 = mfma32(kf[4 + c], B, D1);
                Dm = mfma32(u32x4{km[2 * c].x, km[2 * c].y, km[2 * c + 1].x, km[2 * c + 1].y}, B, Dm);
            }
            float d0, d1, d2, d3, m0, m1, mx, tu;
            asm volatile("s_mov_b32 vcc_lo, 0x00ff00ff\n\t"
                         "s_mov_b32 vcc_hi, 0x00ff00ff\n\t"
                         "s_nop 7\n\t"
                         "v_max3_f32 %0, %8, %9, %10\n\t"
                         "v_max3_f32 %1, %11, %12, %13\n\t"
                         "v_max3_f32 %0, %0, %14, %15\n\t"
                         "v_max3_f32 %1, %1, %16, %17\n\t"
                         "v_max_f32 %0, %0, %1\n\t"
                         "v_mov_b32 %1, %0\n\t"
                         "v_cndmask_b32_dpp %2, %12, %8, vcc row_ror:8 row_mask:0xf bank_mask:0xf\n\t"
                         "v_cndmask_b32_dpp %3, %13, %9, vcc row_ror:8 row_mask:0xf bank_mask:0xf\n\t"
                         "v_permlane16_swap_b32 %0, %1\n\t"
                         "v_max_f32 %0, %0, %1\n\t"
                         "v_mov_b32 %1, %0\n\t"
                         "v_cndmask_b32_dpp %4, %14, %10, vcc row_ror:8 row_mask:0xf bank_mask:0xf\n\t"
                         "v_cndmask_b32_dpp %5, %15, %11, vcc row_ror:8 row_mask:0xf bank_mask:0xf\n\t"
                         "v_permlane32_swap_b32 %0, %1\n\t"
                         "v_max_f32 %0, %0, %1\n\t"
                         "v_mov_b32 %6, %16\n\t"
                         "v_mov_b32 %7, %17\n\t"
                         "v_mov_b32_dpp %0, %0 row_shr:8 row_mask:0xf bank_mask:0xc"
                         : "=&v"(mx), "=&v"(tu), "=&v"(d0), "=&v"(d1), "=&v"(d2), "=&v"(d3), "=&v"(m0), "=&v"(m1)
                         : "v"(D0[0]), "v"(D0[1]), "v"(D0[2]), "v"(D0[3]), "v"(D1[0]), "v"(D1[1]), "v"(D1[2]), "v"(D1[3]), "v"(Dm[0]), "v"(Dm[1])
                         : "vcc");
            const float c = -mx * sc2, cm = c + lmask;
            const float e0 = __builtin_amdgcn_exp2f(__builtin_fmaf(d0, sc2, c)), e1 = __builtin_amdgcn_exp2f(__builtin_fmaf(d1, sc2, c));
            const float e2 = __builtin_amdgcn_exp2f(__builtin_fmaf(d2, sc2, c)), e3 = __builtin_amdgcn_exp2f(__builtin_fmaf(d3, sc2, c));
            const float e4 = __builtin_amdgcn_exp2f(__builtin_fmaf(m0, sc2, cm)), e5 = __builtin_amdgcn_exp2f(__builtin_fmaf(m1, sc2, cm));
            const float sum = q8_sum(((e0 + e1) + (e2 + e3)) + (e4 + e5));
            const float r = __builtin_amdgcn_rcpf(sum);
            const u32x2 ox = {sanit(pk2(e0 * r, e1 * r)), sanit(pk2(e2 * r, e3 * r))};
            const unsigned om = sanit(pk2(e4 * r, e5 * r));
            SCHED0();
            st64(rs, ax, ox);
            st32(rs, am, om);
            asm volatile("" :: "v"(D0), "v"(D1), "v"(Dm));
            SCHED0();
        }
        TSC(4, TSM_S);
        if (s < NSEG - 1) {
            if (s > 0 && w < 2 && lane == 0) st32(rs, OFF_FLAG + (FL_P + h * 32 + w) * 4, (unsigned)s);
            if (avail < (unsigned)(s + 2)) { wait_flags(rs, fqk, true, (unsigned)(s + 2), dead, 2); avail = s + 2; if (dead) cap = 0; }
            kx = ld128(rs, OFF_KX + (((s + 1) * 32 + h) * 8 + w) * 1024 + lane * 16);
            qx = ld128(rs, OFF_QX + (((s + 1) * 32 + h) * 8 + w) * 1024 + lane * 16);
            if (avail < (unsigned)(s + FL_LEAD)) fl = ld32(rs, fqk);
            SCHED0();
        } else {
            if (w < 2 && lane == 0) st32(rs, OFF_FLAG + (FL_P + h * 32 + w) * 4, (unsigned)s);
        }
        TSC(5, TSM_S);
        if (w < 2) {
            f32x4 dm = {0.f, 0.f, 0.f, 0.f}, d0 = dm, d1 = dm;
#pragma unroll
            for (int c = 0; c < 4; ++c) {
                const u32x4 k0 = *(const u32x4*)(kbuf + par * 8192 + c * 1024 + lane * 16);
                const u32x4 k1 = *(const u32x4*)(kbuf + par * 8192 + 4096 + c * 1024 + lane * 16);
                const u32x4 qf = *(const u32x4*)(qbuf + par * 8192 + w * 4096 + c * 1024 + lane * 16);
                const u32x2 ya = trd(yr + (2 * c) * 1280 + 48), yb = trd(yr + (2 * c + 1) * 1280 + 48);
                dm = mfma32(u32x4{ya.x, ya.y, yb.x, yb.y}, qf, dm);
                d0 = mfma32(k0, qf, d0);
                d1 = mfma32(k1, qf, d1);
            }
            const int tok = 16 * w + l16;
            float mx = fmaxf(dm[0], dm[1]) * sc2;
            dm[0] *= sc2; dm[1] *= sc2;
#pragma unroll
            for (int i = 0; i < 4; ++i) {
                d0[i] = (4 * g + i <= tok) ? d0[i] * sc2 : -INFINITY;
                d1[i] = (16 + 4 * g + i <= tok) ? d1[i] * sc2 : -INFINITY;
                mx = fmaxf(mx, fmaxf(d0[i], d1[i]));
            }
            mx = xg_max(mx);
            dm[0] = __builtin_amdgcn_exp2f(dm[0] - mx); dm[1] = __builtin_amdgcn_exp2f(dm[1] - mx);
            float sum = dm[0] + dm[1];
#pragma unroll
            for (int i = 0; i < 4; ++i) {
                d0[i] = __builtin_amdgcn_exp2f(d0[i] - mx); d1[i] = __builtin_amdgcn_exp2f(d1[i] - mx);
                sum += d0[i] + d1[i];
            }
            const float r = __builtin_amdgcn_rcpf(xg_sum(sum));
            st128(rs, OFF_PX + ((s * 32 + h) * 2 + w) * 1024 + lane * 16,
                  u32x4{pk2(d0[0] * r, d0[1] * r), pk2(d0[2] * r, d0[3] * r), pk2(d1[0] * r, d1[1] * r), pk2(d1[2] * r, d1[3] * r)});
            st32(rs, OFF_PM + (s * 32 + h) * 512 + tok * 16 + g * 4, pk2(dm[0] * r, dm[1] * r));
            if (s == NSEG - 1) { WAIT_VM0(); if (lane == 0) st32(rs, OFF_FLAG + (FL_P + h * 32 + w) * 4, (unsigned)NSEG); }
        }
        TSC(6, TSM_S);
    }
    CNT_OUT();
}

DEVI void role_col(const Params& p, rsrc_t rs, char* lds, int tid, int j) {
    const int lane = tid & 63, w = __builtin_amdgcn_readfirstlane(tid >> 6), g = lane >> 4, l16 = lane & 15;
    u32x4 bi[2][16];
    {
        const char* src = p.ws + OFF_BIMG + (size_t)(j * 8 + w) * 32768 + lane * 16;
#pragma unroll
        for (int ii = 0; ii < 2; ++ii)
#pragma unroll
            for (int k = 0; k < 16; ++k) bi[ii][k] = *(const u32x4*)(src + (ii * 16 + k) * 1024);
    }
    char* membuf = lds;
    float* red = (float*)(lds + 8192);
    const int idx = l16 < 8 ? 0x4444 : 0xEEEE;
    bool dead = false;
    const int base = get_base(rs, dead);
    const int fvu = OFF_FLAG + (FL_VU + (4 * w + (lane & 3)) * 32) * 4;
    const int uoff = OFF_UX + ((4 * w + (l16 >> 3)) * 32 + (j >> 1)) * 1024 + (g * 16 + 8 * (j & 1) + (l16 & 7)) * 16;
    const int pmo = w * 1024 + lane * 16;
    const int po0 = amap(2 * w * 1024 + lane * 16);
    const int pom = (4 * w + 2 * (g >> 1) + (l16 >> 3)) * 128 + (l16 & 7) * 16 + (g & 1) * 8;
    const int umo = OFF_UM + ((4 * w + 2 * (g >> 1) + (l16 >> 3)) * 512 + 8 * j + (l16 & 7)) * 16 + (g & 1) * 8;
    float* rw = red + ((4 * (g & 1)) * 8 + (l16 & 7)) * 20 + w * 2 + (g >> 1);
    const int pub = j * 128 + (lane >> 3) * 16 + ((lane & 7) >> 1) * 4;
    unsigned cap = dead ? 0u : SPIN_CAP;
    unsigned fl = 0;
    unsigned tpm = now32(), tpp = tpm;
    CNT_DECL;
    for (int s = 0; s < NSEG; ++s) {
        int o0 = ho_px(base, s) + po0, om = ho_pm(base, s) + pom, off = ho_mem(base, s) + pmo;
        int wp = (lane & 1) == 0 ? ho_mem(base, s + 1) + pub : OOB;
        asm volatile("" : "+v"(o0), "+v"(om), "+v"(wp), "+v"(off));
        nap_until(tpm);
        {
            unsigned sp = 0; bool ok;
            do { const u32x4 v = ld128(rs, off); *(u32x4*)(membuf + w * 1024 + lane * 16) = v; MEMBAR(); ok = __all(okw(v)); ++sp; } while (!ok && sp < cap);
            CNT(0);
            if (__builtin_expect(!ok, 0)) { if (cap) st32(rs, OFF_FLAG + FL_ERR * 4, 4u); cap = 0; }
        }
        TSC(0, TSM_C);
        u32x4 ux0 = {0u, 0u, 0u, 0u}, ux1 = ux0;
        if (s < NSEG - 1) {
            if (!__all(fl >= (unsigned)(s + 1))) { wait_flags(rs, fvu, true, (unsigned)(s + 1), dead, 2); if (dead) cap = 0; }
            ux0 = ld128(rs, uoff + s * MB); ux1 = ld128(rs, uoff + s * MB + 2 * 32768);
            if (!__all(fl >= (unsigned)(s + FL_LEAD))) fl = ld32(rs, fvu);
        }
        SCHED0();
        __syncthreads();
        TSC(1, TSM_C);
        u32x4 a[8];
#pragma unroll
        for (int t = 0; t < 8; ++t) a[t] = *(const u32x4*)(membuf + t * 1024 + lane * 16);
        SCHED0();
        f32x4 a0 = {0.f, 0.f, 0.f, 0.f}, a1 = a0;
#pragma unroll
        for (int t = 0; t < 8; ++t) {
            a0 = smfmac(a[t], bi[0][2 * t], bi[0][2 * t + 1], a0, idx);
            a1 = smfmac(a[t], bi[1][2 * t], bi[1][2 * t + 1], a1, idx);
        }
        f32x4 X;
#pragma unroll
        for (int i = 0; i < 4; ++i) {
            X[i] = fold32(a0[i], a1[i]);
        }
        const u32x2 xb = {pk2(X[0], X[1]), pk2(X[2], X[3])};
        st64(rs, umo + s * (256 * 1024), xb);
        if (s == NSEG - 1) {
            WAIT_VM0();
            __syncthreads();
            if (tid == 0) st32(rs, OFF_FLAG + (FL_U + j * 32) * 4, (unsigned)NSEG);
            CNT_OUT();
            break;
        }
        TSC(2, TSM_C);
        nap_until(tpp);
        f32x4 D;
        {
            unsigned sp = 0; bool pok;
            do {
                const u32x4 p0 = ld128(rs, o0), p1 = ld128(rs, o0 + 1024); const u32x2 pm = ld64(rs, om);
                D = mfma32(p0, ux0, f32x4{0.f, 0.f, 0.f, 0.f});
                const f32x4 Dm = mfma16(pm, xb, f32x4{0.f, 0.f, 0.f, 0.f});
                D = mfma32(p1, ux1, D);
                pok = __all(umax(umax(mx4(p0), mx4(p1)), umax(pm.x, pm.y)) != POISON); ++sp;
                D += Dm;
                MEMBAR();
            } while (!pok && sp < cap);
            CNT(1);
            TSC(3, TSM_C);
            if ((l16 < 8) == (g < 2)) {
#pragma unroll
                for (int i = 0; i < 4; ++i) rw[i * 160] = D[i];
            }
            if (__builtin_expect(!pok, 0)) { if (cap) st32(rs, OFF_FLAG + FL_ERR * 4, 5u); cap = 0; }
        }
        __syncthreads();
        { const unsigned tr = now32(); tpm = tr + DLY_C2; tpp = tr + DLY_P; }
        TSC(4, TSM_C);
        if (w == 0) {
            const f32x4* rp = (const f32x4*)(red + lane * 20);
            const f32x4 r0 = rp[0], r1 = rp[1], r2 = rp[2], r3 = rp[3];
            const f32x4 r4 = (r0 + r1) + (r2 + r3);
            const float sv = (r4[0] + r4[1]) + (r4[2] + r4[3]);
            const unsigned d0 = sanit(pk2(sv, dppf<DPP_XOR1>(sv)));
            st32(rs, wp, d0);
            SCHED0();
            if (lane == 0) st32(rs, OFF_FLAG + (FL_U + j * 32) * 4, (unsigned)(s + 1));
        }
        TSC(5, TSM_C);
    }
}

DEVI void role_qk(const Params& p, rsrc_t rs, char* lds, int tid, int h) {
    const int rot = ROT_Q(h);
    const int lane = tid & 63, w = tid >> 6;
    u32x4 wq[16], wk[16];
    {
        u32x4 v1[16], v2[16];
        stage_load<512, 128>(p.wq + h * 128, 4096, tid, v1, rot);
        stage_load<512, 128>(p.wk + h * 128, 4096, tid, v2, rot);
        SCHED0();
        stage_store<512, 128, false>(v1, lds, tid, rot);
        __syncthreads();
#pragma unroll
        for (int ks = 0; ks < 16; ++ks) wq[ks] = tr_frag<128>(lds, ks, 16 * w, lane);
        __syncthreads();
        stage_store<512, 128, false>(v2, lds, tid, rot);
    }
    __syncthreads();
#pragma unroll
    for (int ks = 0; ks < 16; ++ks) wk[ks] = tr_frag<128>(lds, ks, 16 * w, lane);
    __syncthreads();
    const char* xn = p.ws + OFF_XNF + tid * 16;
    u32x4 xr[4];
#pragma unroll
    for (int i = 0; i < 4; ++i) xr[i] = *(const u32x4*)(xn + i * 8192);
    const int fo = OFF_FLAG + (FL_QK + h * 32) * 4;
    for (int s = 0; s < NSEG; ++s) {
        TS(0);
        char* xb = lds + (s & 1) * 32768;
#pragma unroll
        for (int i = 0; i < 4; ++i) *(u32x4*)(xb + i * 8192 + tid * 16) = xr[i];
        MEMBAR();
        __syncthreads();
        if (s >= FL_EARLY + 2 && s < NSEG - 1 && tid == 0) st32(rs, fo, (unsigned)(s - 1));
        {
            const int sn = s + 1 < NSEG ? s + 1 : s;
#pragma unroll
            for (int i = 0; i < 4; ++i) xr[i] = *(const u32x4*)(xn + sn * 32768 + i * 8192);
        }
        THROTTLE(s);
        SCHED0();
        f32x4 q0 = {0.f, 0.f, 0.f, 0.f}, q1 = q0, k0 = q0, k1 = q0;
        u32x4 fb[3][4];
        const char* xl = xb + lane * 16;
#define QK_LD(c) do { fb[(c) % 3][0] = *(const u32x4*)(xl + (2 * (c)) * 1024); fb[(c) % 3][1] = *(const u32x4*)(xl + 16384 + (2 * (c)) * 1024); \
                      fb[(c) % 3][2] = *(const u32x4*)(xl + (2 * (c) + 1) * 1024); fb[(c) % 3][3] = *(const u32x4*)(xl + 16384 + (2 * (c) + 1) * 1024); } while (0)
        QK_LD(0); QK_LD(1);
#pragma unroll
        for (int c = 0; c < 8; ++c) {
            if (c + 2 < 8) QK_LD(c + 2);
            SCHED0();
            q0 = mfma32(wq[2 * c], fb[c % 3][0], q0); q1 = mfma32(wq[2 * c], fb[c % 3][1], q1);
            k0 = mfma32(wk[2 * c], fb[c % 3][0], k0); k1 = mfma32(wk[2 * c], fb[c % 3][1], k1);
            q0 = mfma32(wq[2 * c + 1], fb[c % 3][2], q0); q1 = mfma32(wq[2 * c + 1], fb[c % 3][3], q1);
            k0 = mfma32(wk[2 * c + 1], fb[c % 3][2], k0); k1 = mfma32(wk[2 * c + 1], fb[c % 3][3], k1);
            SCHED0();
        }
        const int o = ((s * 32 + h) * 8 + w) * 1024 + lane * 16;
        st128(rs, OFF_QX + o, u32x4{pk2(q0[0], q0[1]), pk2(q0[2], q0[3]), pk2(q1[0], q1[1]), pk2(q1[2], q1[3])});
        st128(rs, OFF_KX + o, u32x4{pk2(k0[0], k0[1]), pk2(k0[2], k0[3]), pk2(k1[0], k1[1]), pk2(k1[2], k1[3])});
        TS(1);
        if (s < FL_EARLY || s >= NSEG - 2) { WAIT_VM0(); __syncthreads(); if (tid == 0) st32(rs, fo, (unsigned)(s + 1)); }
    }
}

DEVI void role_vu(const Params& p, rsrc_t rs, char* lds, int tid, int h) {
    const int rot = ROT_Q(h);
    const int lane = tid & 63, w = tid >> 6;
    u32x4 wv[16], wo[16];
    {
        u32x4 v1[16], v2[16];
        stage_load<512, 128>(p.wv + h * 128, 4096, tid, v1, rot);
        stage_load<128, 512>(p.wo + (size_t)h * 128 * 512, 512, tid, v2, rot);
        SCHED0();
        stage_store<512, 128, false>(v1, lds, tid, rot);
        __syncthreads();
#pragma unroll
        for (int ks = 0; ks < 16; ++ks) wv[ks] = tr_frag<128>(lds, ks, 16 * w, lane);
        __syncthreads();
        stage_store<128, 512, false>(v2, lds, tid, rot);
    }
    __syncthreads();
#pragma unroll
    for (int ci = 0; ci < 4; ++ci)
#pragma unroll
        for (int ks = 0; ks < 4; ++ks) wo[ci * 4 + ks] = tr_frag<512>(lds, ks, 16 * (4 * w + ci), lane);
    __syncthreads();
    const char* xn = p.ws + OFF_XNF + tid * 16;
    u32x4 xr[4];
#pragma unroll
    for (int i = 0; i < 4; ++i) xr[i] = *(const u32x4*)(xn + i * 8192);
    char* vbuf = lds + 65536;
    const int fo = OFF_FLAG + (FL_VU + h * 32) * 4;
    for (int s = 0; s < NSEG; ++s) {
        TS(0);
        char* xb = lds + (s & 1) * 32768;
#pragma unroll
        for (int i = 0; i < 4; ++i) *(u32x4*)(xb + i * 8192 + tid * 16) = xr[i];
        MEMBAR();
        __syncthreads();
        if (s >= FL_EARLY + 2 && s < NSEG - 1 && tid == 0) st32(rs, fo, (unsigned)(s - 1));
        {
            const int sn = s + 1 < NSEG ? s + 1 : s;
#pragma unroll
            for (int i = 0; i < 4; ++i) xr[i] = *(const u32x4*)(xn + sn * 32768 + i * 8192);
        }
        THROTTLE(s);
        SCHED0();
        f32x4 v0 = {0.f, 0.f, 0.f, 0.f}, v1 = v0, v0b = v0, v1b = v0;
        u32x4 fb[3][4];
        const char* xl = xb + lane * 16;
        QK_LD(0); QK_LD(1);
#pragma unroll
        for (int c = 0; c < 8; ++c) {
            if (c + 2 < 8) QK_LD(c + 2);
            SCHED0();
            v0 = mfma32(wv[2 * c], fb[c % 3][0], v0); v1 = mfma32(wv[2 * c], fb[c % 3][1], v1);
            v0b = mfma32(wv[2 * c + 1], fb[c % 3][2], v0b); v1b = mfma32(wv[2 * c + 1], fb[c % 3][3], v1b);
            SCHED0();
        }
        v0 += v0b; v1 += v1b;
        *(u32x2*)(vbuf + (w >> 1) * 1024 + lane * 16 + (w & 1) * 8) = u32x2{pk2(v0[0], v0[1]), pk2(v0[2], v0[3])};
        *(u32x2*)(vbuf + 4096 + (w >> 1) * 1024 + lane * 16 + (w & 1) * 8) = u32x2{pk2(v1[0], v1[1]), pk2(v1[2], v1[3])};
        __syncthreads();
        u32x4 va[2][4];
#pragma unroll
        for (int ks = 0; ks < 4; ++ks)
#pragma unroll
            for (int mt = 0; mt < 2; ++mt) va[mt][ks] = *(const u32x4*)(vbuf + mt * 4096 + ks * 1024 + lane * 16);
        SCHED0();
        f32x4 u[4][2];
#pragma unroll
        for (int ci = 0; ci < 4; ++ci)
#pragma unroll
            for (int mt = 0; mt < 2; ++mt) {
                u[ci][mt] = f32x4{0.f, 0.f, 0.f, 0.f};
            }
#pragma unroll
        for (int ks = 0; ks < 4; ++ks)
#pragma unroll
            for (int ci = 0; ci < 4; ++ci)
#pragma unroll
                for (int mt = 0; mt < 2; ++mt) u[ci][mt] = mfma32(va[mt][ks], wo[ci * 4 + ks], u[ci][mt]);
#pragma unroll
        for (int ci = 0; ci < 4; ++ci)
            st128(rs, OFF_UX + s * MB + (h * 32 + 4 * w + ci) * 1024 + lane * 16,
                  u32x4{pk2(u[ci][0][0], u[ci][0][1]), pk2(u[ci][0][2], u[ci][0][3]), pk2(u[ci][1][0], u[ci][1][1]), pk2(u[ci][1][2], u[ci][1][3])});
        TS(1);
        if (s < FL_EARLY || s >= NSEG - 2) { WAIT_VM0(); __syncthreads(); if (tid == 0) st32(rs, fo, (unsigned)(s + 1)); }
    }
}

constexpr u32x4 POIS4 = {POISON, POISON, POISON, POISON};
DEVI u32x4 san4(u32x4 v) { return u32x4{sanit(v.x), sanit(v.y), sanit(v.z), sanit(v.w)}; }
DEVI void init_done(rsrc_t rs, int tid, int wid) {
    WAIT_VM0();
    __syncthreads();
    if (tid == 0) st32(rs, OFF_FLAG + (FL_INIT + wid - 64) * 4, 1u);
}
DEVI void init_wait(rsrc_t rs, int lane, unsigned& cap) {
    for (unsigned sp = 0;; ++sp) {
        const unsigned fa = ld32(rs, OFF_FLAG + (FL_INIT + lane) * 4), fb = ld32(rs, OFF_FLAG + (FL_INIT + 64 + (lane < 12 ? lane : 0)) * 4);
        MEMBAR();
        if (__all(fa != 0u && fb != 0u)) break;
        if (sp >= cap) { if (cap) st32(rs, OFF_FLAG + FL_ERR * 4, 8u); cap = 0; break; }
        __builtin_amdgcn_s_sleep(16);
    }
}
DEVI void probe_word(rsrc_t rs, int off, unsigned& cap) {
    for (unsigned sp = 0;; ++sp) {
        const unsigned v = ld32(rs, off);
        MEMBAR();
        if (__builtin_amdgcn_readfirstlane(v) != POISON) break;
        if (sp >= cap) { if (cap) st32(rs, OFF_FLAG + FL_ERR * 4, 9u); cap = 0; break; }
        __builtin_amdgcn_s_sleep(2);
    }
}
#define RETRY_OR_DIE(code) if (sp >= cap) { if (cap) st32(rs, OFF_FLAG + FL_ERR * 4, code); cap = 0; break; }
#define TAIL_S NSEG
#define PROBE_FIRST 1

DEVI void tb2_iter(const Params& p, rsrc_t rs, f32x4* red, int tid, int i, int s, u32x4 (&ua)[4][2], u32x4 (&un)[4][2], unsigned& flv, bool& dead) {
    const int lane = tid & 63, w = tid >> 6, g = lane >> 4, l16 = lane & 15;
    char* lds = (char*)red; (void)lds;
    u32x2 xl = {0u, 0u}, xh = {0u, 0u};
    if (w < 2) {
        const __bf16* xs = p.x + (size_t)(32 * s + 16 * w + l16) * 512 + 32 * i + 4 * g;
        xl = *(const u32x2*)xs; xh = *(const u32x2*)(xs + 16);
    }
    {
        const int o = lane < 8 ? (FL_P + (4 * w + (lane >> 1)) * 32 + (lane & 1)) : (FL_U + (4 * i + (lane & 3)) * 32);
        wait_flags(rs, OFF_FLAG + o * 4, true, (unsigned)(s + 1), dead, 1);
    }
    TS(1);
    u32x4 pb[4][2], pm[2], um[2];
#pragma unroll
    for (int mt = 0; mt < 2; ++mt) pm[mt] = ld128(rs, OFF_PM + (s * 32 + 4 * w + g) * 512 + (16 * mt + l16) * 16);
#pragma unroll
    for (int ct = 0; ct < 2; ++ct) um[ct] = ld128(rs, OFF_UM + s * (256 * 1024) + ((4 * w + g) * 512 + 32 * i + 16 * ct + l16) * 16);
#pragma unroll
    for (int hh = 0; hh < 4; ++hh)
#pragma unroll
        for (int mt = 0; mt < 2; ++mt) pb[hh][mt] = ld128(rs, OFF_PX + ((s * 32 + 4 * w + hh) * 2 + mt) * 1024 + lane * 16);
    SCHED0();
    const int fvu = OFF_FLAG + (FL_VU + (4 * w + (lane & 3)) * 32) * 4;
    if (s + 2 < NSEG) {
        if (!__all(flv >= (unsigned)(s + 3))) wait_flags(rs, fvu, true, (unsigned)(s + 3), dead, 1);
#pragma unroll
        for (int hh = 0; hh < 4; ++hh)
#pragma unroll
            for (int ct = 0; ct < 2; ++ct) un[hh][ct] = ld128(rs, OFF_UX + (s + 2) * MB + ((4 * w + hh) * 32 + 2 * i + ct) * 1024 + lane * 16);
        if (!__all(flv >= (unsigned)(s + 2 + 2 * FL_LEAD))) flv = ld32(rs, fvu);
    }
    SCHED0();
    f32x4 acc[2][2];
#pragma unroll
    for (int ct = 0; ct < 2; ++ct)
#pragma unroll
        for (int mt = 0; mt < 2; ++mt) acc[ct][mt] = mfma32(um[ct], pm[mt], f32x4{0.f, 0.f, 0.f, 0.f});
#pragma unroll
    for (int hh = 0; hh < 4; ++hh)
#pragma unroll
        for (int ct = 0; ct < 2; ++ct)
#pragma unroll
            for (int mt = 0; mt < 2; ++mt) acc[ct][mt] = mfma32(ua[hh][ct], pb[hh][mt], acc[ct][mt]);
    __syncthreads();
#pragma unroll
    for (int ct = 0; ct < 2; ++ct)
#pragma unroll
        for (int mt = 0; mt < 2; ++mt) red[((w * 2 + ct) * 2 + mt) * 64 + lane] = acc[ct][mt];
    __syncthreads();
    if (w < 2) {
        f32x4 s0 = {0.f, 0.f, 0.f, 0.f}, s1 = s0;
#pragma unroll
        for (int k = 0; k < 8; ++k) { s0 += red[((k * 2 + 0) * 2 + w) * 64 + lane]; s1 += red[((k * 2 + 1) * 2 + w) * 64 + lane]; }
        const u32x4 o = {pk2(bflo(xl.x) + s0[0], bfhi(xl.x) + s0[1]), pk2(bflo(xl.y) + s0[2], bfhi(xl.y) + s0[3]),
                         pk2(bflo(xh.x) + s1[0], bfhi(xh.x) + s1[1]), pk2(bflo(xh.y) + s1[2], bfhi(xh.y) + s1[3])};
        st128(rs, OFF_HF + ((2 * s + w) * 16 + i) * 1024 + lane * 16, san4(o));
    }
    TS(2);
}
DEVI void role_tb2(const Params& p, rsrc_t rs, char* lds, int tid, int par, int i) {
    const int lane = tid & 63, w = tid >> 6;
    f32x4* red = (f32x4*)lds;
    bool dead = false;
#pragma unroll
    for (int k = 0; k < 8; ++k) {
        const int e = w * 8 + k, s = par + 2 * (e >> 1);
        st128(rs, OFF_HF + ((2 * s + (e & 1)) * 16 + i) * 1024 + lane * 16, POIS4);
    }
    init_done(rs, tid, 64 + 2 * i + par);
    u32x4 ua[4][2], ub[4][2];
    const int fvu = OFF_FLAG + (FL_VU + (4 * w + (lane & 3)) * 32) * 4;
    wait_flags(rs, fvu, true, (unsigned)(par + 1), dead, 4);
#pragma unroll
    for (int hh = 0; hh < 4; ++hh)
#pragma unroll
        for (int ct = 0; ct < 2; ++ct) ua[hh][ct] = ld128(rs, OFF_UX + par * MB + ((4 * w + hh) * 32 + 2 * i + ct) * 1024 + lane * 16);
    unsigned flv = 0;
    for (int s = par; s < NSEG; s += 4) {
        tb2_iter(p, rs, red, tid, i, s, ua, ub, flv, dead);
        tb2_iter(p, rs, red, tid, i, s + 2, ub, ua, flv, dead);
    }
}

DEVI float q4_sum(float v) { v += dppf<DPP_ROR4>(v); v += dppf<DPP_ROR8>(v); return xg_sum(v); }
DEVI float dot2(unsigned a, unsigned b, float c) { return __builtin_amdgcn_fdot2_f32_bf16(__builtin_bit_cast(bf16x2, a), __builtin_bit_cast(bf16x2, b), c, false); }
DEVI void wave_ln(u32x4 (&fr)[4]) {
    float a[2] = {0.f, 0.f}, q[2] = {0.f, 0.f};
#pragma unroll
    for (int g = 0; g < 4; ++g)
#pragma unroll
        for (int k = 0; k < 4; ++k) { a[k & 1] = dot2(fr[g][k], 0x3F803F80u, a[k & 1]); q[k & 1] = dot2(fr[g][k], fr[g][k], q[k & 1]); }
    const float mean = q4_sum(a[0] + a[1]) * (1.0f / 512.0f);
    const float var = fmaxf(q4_sum(q[0] + q[1]) * (1.0f / 512.0f) - mean * mean, 0.f);
    const float rstd = __builtin_amdgcn_rsqf(var + 1e-5f);
    const f32x2 r2 = {rstd, rstd}, m2 = {mean, mean};
#pragma unroll
    for (int g = 0; g < 4; ++g)
#pragma unroll
        for (int k = 0; k < 4; ++k) {
            const f32x2 v = (f32x2{bflo(fr[g][k]), bfhi(fr[g][k])} - m2) * r2;
            fr[g][k] = pk2(v[0], v[1]);
        }
}

template <int MODE>
DEVI void role_mlp(const Params& p, rsrc_t rs, char* lds, int tid, int par, int i) {
    const int lane = tid & 63, w = tid >> 6, g = lane >> 4, l16 = lane & 15;
    const int pr = w >> 1, mt = w & 1;
    u32x4 wa[2][16];
    {
        const __bf16* wsrc = MODE == 1 ? p.w1 : (MODE == 2 ? p.w2 : p.w3);
        constexpr int LD = MODE == 3 ? 1792 : 512;
        stage_rows<512, 128, false>(wsrc + 128 * i, LD, lds, tid);
        __syncthreads();
#pragma unroll
        for (int nt = 0; nt < 2; ++nt)
#pragma unroll
            for (int ks = 0; ks < 16; ++ks) wa[nt][ks] = tr_frag<128>(lds, ks, 32 * pr + 16 * nt, lane);
        __syncthreads();
    }
    const int fo = ((w >> 2) * 16 + (lane >> 2)) * 1024 + (4 * (w & 3) + (lane & 3)) * 16;
    for (int s = par; s < NSEG; s += 2) {
        if (MODE == 1) {
            st128(rs, OFF_H1F + ((2 * s + mt) * 16 + 4 * i + pr) * 1024 + lane * 16, POIS4);
            st128(rs, OFF_YF + ((2 * s + mt) * 16 + 4 * i + pr) * 1024 + lane * 16, POIS4);
        } else if (MODE == 2) st128(rs, OFF_X1F + ((2 * s + mt) * 16 + 4 * i + pr) * 1024 + lane * 16, POIS4);
        else st128(rs, OFF_AF + ((2 * s + mt) * 56 + 4 * i + pr) * 1024 + lane * 16, POIS4);
    }
    init_done(rs, tid, (MODE == 1 ? 96 : (MODE == 2 ? 104 : 112)) + 2 * i + par);
    unsigned cap = SPIN_CAP;
    init_wait(rs, lane, cap);
    for (int s = par; s < NSEG; s += 2) {
        char* zb = lds + ((s >> 1) & 1) * 32768;
        u32x4 yv = {0u, 0u, 0u, 0u};
        if (MODE == 1 || MODE == 3) {
            u32x4 fr[4];
            const int src = (MODE == 1 ? OFF_HF : OFF_X1F) + 2 * s * 16 * 1024;
            if (PROBE_FIRST && s < TAIL_S) probe_word(rs, src + ((w >> 2) * 16 + 15) * 1024 + (48 + 4 * (w & 3) + 3) * 16 + 12, cap);
            for (unsigned sp = 0;; ++sp) {
#pragma unroll
                for (int k = 0; k < 4; ++k) fr[k] = ld128(rs, src + fo + k * 256);
                MEMBAR();
                if (__all(umax(umax(mx4(fr[0]), mx4(fr[1])), umax(mx4(fr[2]), mx4(fr[3]))) != POISON)) break;
                RETRY_OR_DIE(10u);
                if (!PROBE_FIRST && s < TAIL_S) probe_word(rs, src + ((w >> 2) * 16 + 15) * 1024 + (48 + 4 * (w & 3) + 3) * 16 + 12, cap);
            }
            wave_ln(fr);
            if (MODE == 1) {
#pragma unroll
                for (int k = 0; k < 4; ++k) st128(rs, (lane >> 4) == i ? OFF_YF + 2 * s * 16 * 1024 + fo + k * 256 : OOB, san4(fr[k]));
                wave_ln(fr);
            }
            TS(1);
#pragma unroll
            for (int k = 0; k < 4; ++k) *(u32x4*)(zb + fo + k * 256) = fr[k];
        } else {
            u32x4 fr[2][2];
            const int q = w >> 1;
            const int dl = q == 0 ? -1 : (q == 1 ? 1 : (q == 2 ? -2 : 2));
            if (PROBE_FIRST && s < TAIL_S) {
                const int T = 32 * s + 31 + dl;
                probe_word(rs, OFF_H1F + ((T >> 4) * 16 + 2 * w + 1) * 1024 + (48 + (T & 15)) * 16 + 12, cap);
            }
            for (unsigned sp = 0;; ++sp) {
                unsigned mxv = 0;
#pragma unroll
                for (int m = 0; m < 2; ++m) {
                    const int T = 32 * s + 16 * m + l16 + dl;
                    const bool ok = T >= 0 && T < 2048;
                    const int Tc = ok ? T : 32 * s;
#pragma unroll
                    for (int k = 0; k < 2; ++k) {
                        u32x4 v = ld128(rs, OFF_H1F + ((Tc >> 4) * 16 + 2 * w + k) * 1024 + (16 * g + (Tc & 15)) * 16);
                        mxv = umax(mxv, mx4(v));
                        fr[m][k] = ok ? v : u32x4{0u, 0u, 0u, 0u};
                    }
                }
                yv = ld128(rs, OFF_YF + ((2 * s + mt) * 16 + 4 * i + pr) * 1024 + lane * 16);
                MEMBAR();
                if (__all(umax(mxv, mx4(yv)) != POISON)) break;
                RETRY_OR_DIE(11u);
                if (!PROBE_FIRST && s < TAIL_S) {
                    const int T = 32 * s + 31 + dl;
                    probe_word(rs, OFF_H1F + ((T >> 4) * 16 + 2 * w + 1) * 1024 + (48 + (T & 15)) * 16 + 12, cap);
                }
            }
            TS(1);
#pragma unroll
            for (int m = 0; m < 2; ++m)
#pragma unroll
                for (int k = 0; k < 2; ++k) *(u32x4*)(zb + (m * 16 + 2 * w + k) * 1024 + lane * 16) = fr[m][k];
        }
        __syncthreads();
        f32x4 a0 = {0.f, 0.f, 0.f, 0.f}, a1 = a0;
        {
            u32x4 fb[3][2];
            const char* zl = zb + mt * 16384 + lane * 16;
#define ML_LD(c) do { fb[(c) % 3][0] = *(const u32x4*)(zl + (2 * (c)) * 1024); fb[(c) % 3][1] = *(const u32x4*)(zl + (2 * (c) + 1) * 1024); } while (0)
            ML_LD(0); ML_LD(1);
#pragma unroll
            for (int c = 0; c < 8; ++c) {
                if (c + 2 < 8) ML_LD(c + 2);
                SCHED0();
                a0 = mfma32(wa[0][2 * c], fb[c % 3][0], a0);
                a1 = mfma32(wa[1][2 * c], fb[c % 3][0], a1);
                a0 = mfma32(wa[0][2 * c + 1], fb[c % 3][1], a0);
                a1 = mfma32(wa[1][2 * c + 1], fb[c % 3][1], a1);
                SCHED0();
            }
        }
        if (MODE == 2) {
            const u32x4 o = {pk2(bflo(yv.x) + rbf(a0[0]), bfhi(yv.x) + rbf(a0[1])), pk2(bflo(yv.y) + rbf(a0[2]), bfhi(yv.y) + rbf(a0[3])),
                             pk2(bflo(yv.z) + rbf(a1[0]), bfhi(yv.z) + rbf(a1[1])), pk2(bflo(yv.w) + rbf(a1[2]), bfhi(yv.w) + rbf(a1[3]))};
            st128(rs, OFF_X1F + ((2 * s + mt) * 16 + 4 * i + pr) * 1024 + lane * 16, san4(o));
        } else {
            const u32x4 o = {pk2(gelu_t(rbf(a0[0])), gelu_t(rbf(a0[1]))), pk2(gelu_t(rbf(a0[2])), gelu_t(rbf(a0[3]))),
                             pk2(gelu_t(rbf(a1[0])), gelu_t(rbf(a1[1]))), pk2(gelu_t(rbf(a1[2])), gelu_t(rbf(a1[3])))};
            if (MODE == 1) st128(rs, OFF_H1F + ((2 * s + mt) * 16 + 4 * i + pr) * 1024 + lane * 16, san4(o));
            else st128(rs, OFF_AF + ((2 * s + mt) * 56 + 4 * i + pr) * 1024 + lane * 16, san4(o));
        }
        TS(2);
    }
}

DEVI void role_m4(const Params& p, rsrc_t rs, char* lds, int tid, int par, int i) {
    const int lane = tid & 63, w = tid >> 6, g = lane >> 4, l16 = lane & 15;
    u32x4 wa[4][7];
#pragma unroll
    for (int r = 0; r < 2; ++r) {
        stage_rows<896, 64, false>(p.w4 + (size_t)(896 * r) * 512 + 64 * i, 512, lds, tid);
        __syncthreads();
        if ((w >> 2) == r) {
#pragma unroll
            for (int nt = 0; nt < 4; ++nt)
#pragma unroll
                for (int k = 0; k < 7; ++k) wa[nt][k] = tr_frag<64>(lds, 7 * (w & 3) + k, 16 * nt, lane);
        }
        __syncthreads();
    }
    unsigned cap = SPIN_CAP;
    init_wait(rs, lane, cap);
    const int nt = w & 3, m = w >> 2;
    for (int s = par; s < NSEG; s += 2) {
        f32x4* red = (f32x4*)(lds + ((s >> 1) & 1) * 65536);
        u32x4 ar[2][7];
        const int src = OFF_AF + s * 114688 + 7 * w * 1024;
        if (PROBE_FIRST && s < TAIL_S) probe_word(rs, src + (56 + 6) * 1024 + 1020, cap);
        for (unsigned sp = 0;; ++sp) {
            unsigned mxv = 0;
#pragma unroll
            for (int mm = 0; mm < 2; ++mm)
#pragma unroll
                for (int k = 0; k < 7; ++k) ar[mm][k] = ld128(rs, src + (mm * 56 + k) * 1024 + lane * 16);
            MEMBAR();
#pragma unroll
            for (int mm = 0; mm < 2; ++mm)
#pragma unroll
                for (int k = 0; k < 7; ++k) mxv = umax(mxv, mx4(ar[mm][k]));
            if (__all(mxv != POISON)) break;
            RETRY_OR_DIE(12u);
            if (!PROBE_FIRST && s < TAIL_S) probe_word(rs, src + (56 + 6) * 1024 + 1020, cap);
        }
        TS(1);
        const int o = ((2 * s + m) * 16 + 2 * i + (nt >> 1)) * 1024 + lane * 16 + (nt & 1) * 8;
        const u32x2 x1v = ld64(rs, OFF_X1F + o), hv = ld64(rs, OFF_HF + o);
        f32x4 acc[4][2];
#pragma unroll
        for (int t = 0; t < 4; ++t) { acc[t][0] = f32x4{0.f, 0.f, 0.f, 0.f}; acc[t][1] = acc[t][0]; }
#pragma unroll
        for (int k = 0; k < 7; ++k)
#pragma unroll
            for (int t = 0; t < 4; ++t)
#pragma unroll
                for (int mm = 0; mm < 2; ++mm) acc[t][mm] = mfma32(wa[t][k], ar[mm][k], acc[t][mm]);
#pragma unroll
        for (int t = 0; t < 4; ++t)
#pragma unroll
            for (int mm = 0; mm < 2; ++mm) red[((w * 4 + t) * 2 + mm) * 64 + lane] = acc[t][mm];
        __syncthreads();
        f32x4 d = red[(nt * 2 + m) * 64 + lane];
#pragma unroll
        for (int k = 1; k < 8; ++k) d += red[((k * 4 + nt) * 2 + m) * 64 + lane];
        const float t0 = rbf(bflo(x1v.x) + rbf(d[0])), t1 = rbf(bfhi(x1v.x) + rbf(d[1]));
        const float t2 = rbf(bflo(x1v.y) + rbf(d[2])), t3 = rbf(bfhi(x1v.y) + rbf(d[3]));
        const u32x2 ov = {pk2(bflo(hv.x) + t0, bfhi(hv.x) + t1), pk2(bflo(hv.y) + t2, bfhi(hv.y) + t3)};
        *(u32x2*)(p.out + (size_t)(32 * s + 16 * m + l16) * 512 + 64 * i + 16 * nt + 4 * g) = ov;
        TS(2);
    }
}

#define DLY_START 450
DEVI void late_start() {
    const unsigned t0 = (unsigned)__builtin_amdgcn_s_memrealtime();
    while ((unsigned)__builtin_amdgcn_s_memrealtime() - t0 < DLY_START) __builtin_amdgcn_s_sleep(32);
}
__global__ void __launch_bounds__(512, 2) main_kernel(Params p) {
    extern __shared__ __attribute__((aligned(16))) char lds[];
    const int tid = threadIdx.x, b = blockIdx.x;
    const rsrc_t rs = __builtin_amdgcn_make_buffer_rsrc(p.ws, 0, WS_SPAN, 0x00020000);
#define XPERM 0xE4
    const int xcd = (b & 4) ? (b & 7) : ((XPERM >> (2 * (b & 3))) & 3), slot = b >> 3;
#define MAP 2
    int role, wid = 0;
    if (MAP == 2 && xcd < 4) {
        if (xcd < 2) { role = 1; wid = slot * 2 + xcd; }
        else if (slot < 16) { role = 0; wid = slot * 2 + (xcd - 2); }
        else { const int idx = (slot - 16) * 2 + (xcd - 2); role = 2; wid = idx == 3 ? 155 : 96 + (idx < 3 ? idx : idx - 1); }
    } else if (MAP == 3 && xcd < 4) {
        if (xcd == 2) { role = 0; wid = slot; }
        else if (slot < 21 || (slot == 21 && xcd == 0)) { role = 1; wid = slot * 3 + (xcd == 3 ? 2 : xcd); }
        else { const int idx = (slot - 21) * 3 + (xcd == 3 ? 2 : xcd) - 1; role = 2; wid = idx == 1 ? 155 : 96 + (idx < 1 ? idx : idx - 1); }
    } else if (xcd < 4) {
        if (slot < 8) { role = 0; wid = slot * 4 + xcd; }
        else if (slot < 24) { role = 1; wid = (slot - 8) * 4 + xcd; }
        else { const int idx = (slot - 24) * 4 + xcd; role = 2; wid = idx == 3 ? 155 : 96 + (idx < 3 ? idx : idx - 1); }
    } else {
        const int q = (slot & 15) * 4 + (xcd - 4);
        role = 2;
        if (slot < 16) wid = q;
        else wid = q < 32 ? 64 + q : (q < 60 ? 127 + (q - 32) : (q == 60 ? 156 : 157));
    }
    if (role == 0) role_score(p, rs, lds, tid, wid);
    else if (role == 1) role_col(p, rs, lds, tid, wid);
    else {
        if (wid == 155) role_probe(rs, lds, tid);
        else if (wid < 32) role_qk(p, rs, lds, tid, wid);
        else if (wid < 64) role_vu(p, rs, lds, tid, wid - 32);
        else if (late_start(), wid < 96) role_tb2(p, rs, lds, tid, (wid - 64) & 1, (wid - 64) >> 1);
        else if (wid < 104) role_mlp<1>(p, rs, lds, tid, (wid - 96) & 1, (wid - 96) >> 1);
        else if (wid < 112) role_mlp<2>(p, rs, lds, tid, (wid - 104) & 1, (wid - 104) >> 1);
        else if (wid < 140) role_mlp<3>(p, rs, lds, tid, (wid - 112) & 1, (wid - 112) >> 1);
        else if (wid < 155) role_m4(p, rs, lds, tid, (wid - 140) & 1, (wid - 140) >> 1);
        else if (wid == 156) role_m4(p, rs, lds, tid, 1, 7);
    }
}

extern "C" void kernel_launch(void* const* d_in, const int* in_sizes, int n_in, void* d_out, int out_size, void* d_ws, size_t ws_size,
                              hipStream_t stream) {
    (void)in_sizes; (void)n_in; (void)out_size; (void)ws_size;
    Params p;
    p.x = (const __bf16*)d_in[0]; p.mem0 = (const __bf16*)d_in[1];
    p.wq = (const __bf16*)d_in[2]; p.wk = (const __bf16*)d_in[3]; p.wv = (const __bf16*)d_in[4]; p.wo = (const __bf16*)d_in[5];
    p.w1 = (const __bf16*)d_in[8]; p.w2 = (const __bf16*)d_in[9]; p.w3 = (const __bf16*)d_in[10]; p.w4 = (const __bf16*)d_in[11];
    p.out = (__bf16*)d_out;
    p.ws = (char*)(((uintptr_t)d_ws + 2097151u) & ~(uintptr_t)2097151u);
    hipFuncSetAttribute((const void*)prep_kernel, hipFuncAttributeMaxDynamicSharedMemorySize, LDS_BYTES);
    hipFuncSetAttribute((const void*)main_kernel, hipFuncAttributeMaxDynamicSharedMemorySize, LDS_BYTES);
    hipLaunchKernelGGL(prep_kernel, dim3(256), dim3(512), LDS_BYTES, stream, p);
    hipLaunchKernelGGL(main_kernel, dim3(256), dim3(512), LDS_BYTES, stream, p);
}
```
